```python
import torch

def _softmax(x, axis=-1):
    return torch.softmax(x, dim=axis)
def _gelu(x):
    return torch.nn.functional.gelu(x, approximate="tanh")
def _silu(x):
    return torch.nn.functional.silu(x)
def _sigmoid(x):
    return torch.sigmoid(x)
def _layernorm(x, axis=-1, eps=1e-5):
    m = x.mean(dim=axis, keepdim=True)
    v = x.var(dim=axis, keepdim=True, unbiased=False)
    return (x - m) / torch.sqrt(v + eps)
def _scatter_add(tgt, idx, src, axis=0):
    return tgt.clone().index_add_(axis, idx, src)
def _segment_sum(src, seg, n):
    out = torch.zeros((n,) + src.shape[1:], dtype=src.dtype)
    return out.index_add_(0, seg, src)
def _pad(x, pw):
    flat = tuple(v for pair in reversed(pw) for v in pair)
    return torch.nn.functional.pad(x, flat)

kernel_name = 'gated_mix__compressed_kv_cla__geglu_ffn__decode'

def setup_inputs():

    _w = (lambda a: (lambda t: t.to(torch.bfloat16) if t.is_floating_point() else t)(torch.as_tensor(a)))
    def _A_setup():
        B, H, Sq, Sk, D, M = 2, 32, 1, 8192, 128, 512
        Dc = 4 * D
        return {
            "x": _w((lambda *s: torch.randn(*s, dtype=torch.bfloat16) * 0.05)(B, Sq, M)),
            "c_kv": _w((lambda *s: torch.randn(*s, dtype=torch.bfloat16) * 1.6)(B, Sk, Dc)),
            "w_q": _w((lambda *s: torch.randn(*s, dtype=torch.bfloat16) * 6.4)(M, H, D) * (M ** -0.5)),
            "w_uk": _w((lambda *s: torch.randn(*s, dtype=torch.bfloat16) * 6.4)(Dc, H, D) * (Dc ** -0.5)),
            "w_uv": _w((lambda *s: torch.randn(*s, dtype=torch.bfloat16) * 1.6)(Dc, H, D) * (Dc ** -0.5)),
            "w_o": _w((lambda *s: torch.randn(*s, dtype=torch.bfloat16) * 1.6)(H, D, M) * ((H * D) ** -0.5)),
        }
    _A_in = _A_setup()

    def _B_setup():
        B, S, D, F = 2, 1, 512, 1792
        return {
            "x": _w((lambda *s: torch.randn(*s, dtype=torch.bfloat16) * 0.05)(B, S, D)),
            "w_gate": _w((lambda *s: torch.randn(*s, dtype=torch.bfloat16) * 0.4)(D, F)),
            "w_up": _w((lambda *s: torch.randn(*s, dtype=torch.bfloat16) * 0.4)(D, F)),
            "w_down": _w((lambda *s: torch.randn(*s, dtype=torch.bfloat16) * 0.4)(F, D)),
        }
    _B_in = _B_setup()

    return {
        "A_x": _A_in["x"],
        "A_c_kv": _A_in["c_kv"],
        "A_w_q": _A_in["w_q"],
        "A_w_uk": _A_in["w_uk"],
        "A_w_uv": _A_in["w_uv"],
        "A_w_o": _A_in["w_o"],
        "B_x": _B_in["x"],
        "B_w_gate": _B_in["w_gate"],
        "B_w_up": _B_in["w_up"],
        "B_w_down": _B_in["w_down"],
        "gate_w": _w((lambda *s: torch.randn(*s, dtype=torch.bfloat16) * 0.1)(512) * 16.0),
    }

def reference(A_x, A_c_kv, A_w_q, A_w_uk, A_w_uv, A_w_o, B_x, B_w_gate, B_w_up, B_w_down, gate_w):
    def _A_ref(x, c_kv, w_q, w_uk, w_uv, w_o):
        B, Sq, M = x.shape[0], x.shape[1], x.shape[2]
        Sk = c_kv.shape[1]
        H, D = w_q.shape[1], w_q.shape[2]
        q = torch.einsum("bqm,mhd->bhqd", x, w_q)
        k = torch.einsum("bkc,chd->bhkd", c_kv, w_uk)
        v = torch.einsum("bkc,chd->bhkd", c_kv, w_uv)
        scores = torch.einsum("bhqd,bhkd->bhqk", q, k) * (D ** -0.5)
        off = Sk - Sq
        qi = torch.arange(Sq)[:, None]
        kj = torch.arange(Sk)[None, :]
        cmask = kj <= (qi + off)
        scores = torch.where(cmask[None, None], scores, -1e9)
        p = _softmax(scores, axis=-1)
        ctx = torch.einsum("bhqk,bhkd->bhqd", p, v)
        return torch.einsum("bhqd,hdm->bqm", ctx, w_o)

    def _B_ref(x, w_gate, w_up, w_down):
        return (_gelu(x @ w_gate) * (x @ w_up)) @ w_down

    x = A_x
    g = _sigmoid((x * gate_w).sum(-1, keepdims=True))
    a = _A_ref(x, A_c_kv, A_w_q, A_w_uk, A_w_uv, A_w_o)
    b = _B_ref(x, B_w_gate, B_w_up, B_w_down)
    return g * a + (1 - g) * b
```

```cpp
#include <hip/hip_runtime.h>
#include <hip/hip_bf16.h>
#include <stdint.h>

typedef unsigned short bf16_t;
__device__ __forceinline__ float bf2f(bf16_t v) { return __uint_as_float(((unsigned)v) << 16); }
__device__ __forceinline__ bf16_t f2bf(float f) { unsigned u = __float_as_uint(f); u += 0x7fffu + ((u >> 16) & 1u); return (bf16_t)(u >> 16); }

constexpr int B = 2, H = 32, SK = 8192, D = 128, M = 512, DC = 512, F = 1792;

__global__ void k_q(const bf16_t* x, const bf16_t* wq, const bf16_t* wg, const bf16_t* wu, float* q, float* hbuf) {
    int col = blockIdx.x * 256 + threadIdx.x;
    if (col < H * D) {
        float a0 = 0, a1 = 0;
        for (int m = 0; m < M; ++m) { float w = bf2f(wq[(size_t)m * H * D + col]); a0 += bf2f(x[m]) * w; a1 += bf2f(x[M + m]) * w; }
        q[col] = a0; q[H * D + col] = a1;
    } else if (col < H * D + F) {
        int f = col - H * D;
        float g0 = 0, g1 = 0, u0 = 0, u1 = 0;
        for (int m = 0; m < M; ++m) { float w = bf2f(wg[(size_t)m * F + f]); float v = bf2f(wu[(size_t)m * F + f]);
            g0 += bf2f(x[m]) * w; g1 += bf2f(x[M + m]) * w; u0 += bf2f(x[m]) * v; u1 += bf2f(x[M + m]) * v; }
        auto gelu = [](float t) { return 0.5f * t * (1.f + tanhf(0.7978845608028654f * (t + 0.044715f * t * t * t))); };
        hbuf[f] = gelu(g0) * u0; hbuf[F + f] = gelu(g1) * u1;
    }
}
__global__ void k_qa(const float* q, const bf16_t* wuk, float* qa) {
    int idx = blockIdx.x * 256 + threadIdx.x;
    int h = idx / DC, c = idx % DC;
    float a0 = 0, a1 = 0;
    for (int d = 0; d < D; ++d) { float w = bf2f(wuk[((size_t)c * H + h) * D + d]); a0 += q[h * D + d] * w; a1 += q[H * D + h * D + d] * w; }
    qa[(0 * H + h) * DC + c] = a0 * 0.08838834764831845f; qa[(1 * H + h) * DC + c] = a1 * 0.08838834764831845f;
}
__global__ void k_s(const float* qa, const bf16_t* ckv, float* s) {
    int k = blockIdx.x * 256 + threadIdx.x; int h = blockIdx.y, b = blockIdx.z;
    __shared__ float sq[DC];
    for (int i = threadIdx.x; i < DC; i += 256) sq[i] = qa[(b * H + h) * DC + i];
    __syncthreads();
    float a = 0;
    const bf16_t* row = ckv + ((size_t)b * SK + k) * DC;
    for (int c = 0; c < DC; ++c) a += sq[c] * bf2f(row[c]);
    s[((size_t)b * H + h) * SK + k] = a;
}
__global__ void k_sm(float* s) {
    float* row = s + (size_t)blockIdx.x * SK;
    __shared__ float red[256];
    float mx = -1e30f;
    for (int i = threadIdx.x; i < SK; i += 256) mx = fmaxf(mx, row[i]);
    red[threadIdx.x] = mx; __syncthreads();
    for (int o = 128; o > 0; o >>= 1) { if (threadIdx.x < o) red[threadIdx.x] = fmaxf(red[threadIdx.x], red[threadIdx.x + o]); __syncthreads(); }
    mx = red[0]; __syncthreads();
    float sum = 0;
    for (int i = threadIdx.x; i < SK; i += 256) { float e = __expf(row[i] - mx); row[i] = e; sum += e; }
    red[threadIdx.x] = sum; __syncthreads();
    for (int o = 128; o > 0; o >>= 1) { if (threadIdx.x < o) red[threadIdx.x] += red[threadIdx.x + o]; __syncthreads(); }
    float inv = 1.f / red[0];
    for (int i = threadIdx.x; i < SK; i += 256) row[i] *= inv;
}
__global__ void k_o(const float* p, const bf16_t* ckv, float* o) {
    int c = blockIdx.x * 256 + threadIdx.x; int h = blockIdx.y, b = blockIdx.z;
    const float* pr = p + ((size_t)b * H + h) * SK;
    float a = 0;
    for (int k = 0; k < SK; ++k) a += pr[k] * bf2f(ckv[((size_t)b * SK + k) * DC + c]);
    o[(b * H + h) * DC + c] = a;
}
__global__ void k_ctx(const float* o, const bf16_t* wuv, float* ctx) {
    int idx = blockIdx.x * 256 + threadIdx.x; int h = idx / D;
    float a0 = 0, a1 = 0;
    for (int c = 0; c < DC; ++c) { float w = bf2f(wuv[(size_t)c * H * D + idx]); a0 += o[h * DC + c] * w; a1 += o[(H + h) * DC + c] * w; }
    ctx[idx] = a0; ctx[H * D + idx] = a1;
}
__global__ void k_out(const float* ctx, const bf16_t* wo, const float* hbuf, const bf16_t* wd, const bf16_t* x, const bf16_t* gw, bf16_t* out) {
    int m = blockIdx.x * 64 + threadIdx.x; int b = blockIdx.y;
    float a = 0, f = 0, g = 0;
    for (int i = 0; i < H * D; ++i) a += ctx[b * H * D + i] * bf2f(wo[(size_t)i * M + m]);
    for (int i = 0; i < F; ++i) f += hbuf[b * F + i] * bf2f(wd[(size_t)i * M + m]);
    for (int i = 0; i < M; ++i) g += bf2f(x[b * M + i]) * bf2f(gw[i]);
    g = 1.f / (1.f + __expf(-g));
    out[b * M + m] = f2bf(g * a + (1.f - g) * f);
}

extern "C" void kernel_launch(void* const* d_in, const int* in_sizes, int n_in, void* d_out, int out_size, void* d_ws, size_t ws_size, hipStream_t stream) {
    const bf16_t* x = (const bf16_t*)d_in[0]; const bf16_t* ckv = (const bf16_t*)d_in[1]; const bf16_t* wq = (const bf16_t*)d_in[2];
    const bf16_t* wuk = (const bf16_t*)d_in[3]; const bf16_t* wuv = (const bf16_t*)d_in[4]; const bf16_t* wo = (const bf16_t*)d_in[5];
    const bf16_t* wg = (const bf16_t*)d_in[7]; const bf16_t* wu = (const bf16_t*)d_in[8]; const bf16_t* wd = (const bf16_t*)d_in[9]; const bf16_t* gw = (const bf16_t*)d_in[10];
    float* ws = (float*)d_ws;
    float* q = ws; float* hbuf = q + 2 * 4096; float* qa = hbuf + 4096; float* s = qa + 2 * 32 * 512; float* o = s + (size_t)2 * 32 * 8192; float* ctx = o + 2 * 32 * 512;
    k_q<<<(H * D + F + 255) / 256, 256, 0, stream>>>(x, wq, wg, wu, q, hbuf);
    k_qa<<<H * DC / 256, 256, 0, stream>>>(q, wuk, qa);
    k_s<<<dim3(SK / 256, H, B), 256, 0, stream>>>(qa, ckv, s);
    k_sm<<<B * H, 256, 0, stream>>>(s);
    k_o<<<dim3(DC / 256, H, B), 256, 0, stream>>>(s, ckv, o);
    k_ctx<<<H * D / 256, 256, 0, stream>>>(o, wuv, ctx);
    k_out<<<dim3(M / 64, B), 64, 0, stream>>>(ctx, wo, hbuf, wd, x, gw, (bf16_t*)d_out);
}
```

```cpp
#include <hip/hip_runtime.h>
#include <stdint.h>

#define GAS __attribute__((address_space(1)))
#define LAS __attribute__((address_space(3)))
typedef unsigned short bf16_t;
typedef unsigned v4u __attribute__((ext_vector_type(4)));
typedef unsigned v2u __attribute__((ext_vector_type(2)));
typedef float f32x4 __attribute__((ext_vector_type(4)));
typedef float f32x2 __attribute__((ext_vector_type(2)));
typedef short bf16x8 __attribute__((ext_vector_type(8)));
typedef short v4s __attribute__((ext_vector_type(4)));

#define STAMPS 0
#define NB0 6
#define NB1 6

constexpr int NWG = 256, NT = 512;
constexpr unsigned WS_EPOCH = 0;
constexpr unsigned WS_E1 = 4096;
constexpr unsigned WS_E2 = WS_E1 + 32768;
constexpr unsigned WS_E4S = WS_E2 + 131072;
constexpr unsigned WS_E5 = WS_E4S + 65536;
constexpr unsigned WS_E6 = WS_E5 + 524288;
constexpr unsigned WS_E4 = WS_E6 + 2097152;
constexpr unsigned WS_END = WS_E4 + 8388608;
constexpr unsigned WS_STAMP = WS_END;

constexpr int L_C = 0, L_Q = 131072, L_P = L_Q + 16384, P_STR = 272, L_RED = L_P + 16 * P_STR, L_MISC = L_RED + 4096, L_TOTAL = L_MISC + 2048;
static_assert(L_TOTAL <= 163840, "LDS");
constexpr int SPIN_CAP = 200000;

__device__ __forceinline__ float bflo(unsigned u) { return __uint_as_float(u << 16); }
__device__ __forceinline__ float bfhi(unsigned u) { return __uint_as_float(u & 0xffff0000u); }
__device__ __forceinline__ float bf2f(bf16_t v) { return __uint_as_float(((unsigned)v) << 16); }
__device__ __forceinline__ unsigned pk_bf16(float lo, float hi) {
    typedef __bf16 bf2 __attribute__((ext_vector_type(2)));
    bf2 r; r[0] = (__bf16)lo; r[1] = (__bf16)hi; return __builtin_bit_cast(unsigned, r);
}
__device__ __forceinline__ unsigned sig(unsigned r) { return ((r & 3u) << 1) | ((((r >> 2) ^ (r >> 3)) & 1u) << 3) | (r >> 3); }
__device__ __forceinline__ f32x4 mfma16(v4u a, v4u b, f32x4 c) {
    return __builtin_amdgcn_mfma_f32_16x16x32_bf16(__builtin_bit_cast(bf16x8, a), __builtin_bit_cast(bf16x8, b), c, 0, 0, 0);
}
__device__ __forceinline__ void glds16(const void* gsrc, unsigned lds_dst) {
    unsigned keep;
    asm volatile("s_mov_b32 %0, m0\n\ts_mov_b32 m0, %2\n\ts_nop 0\n\tglobal_load_lds_dwordx4 %1, off\n\ts_mov_b32 m0, %0"
                 : "=&s"(keep) : "v"(gsrc), "s"(lds_dst) : "memory");
}
#define BAR() do { asm volatile("s_waitcnt lgkmcnt(0)" ::: "memory"); __builtin_amdgcn_s_barrier(); asm volatile("" ::: "memory"); } while (0)
#define PIN() do { asm volatile("" ::: "memory"); __builtin_amdgcn_sched_barrier(0); } while (0)

template <int N>
__device__ __forceinline__ void poll_units(__amdgpu_buffer_rsrc_t rs, const unsigned (&off)[N], v4u (&u)[N], unsigned ta, unsigned tb) {
    for (int spin = 0; spin < SPIN_CAP; ++spin) {
        asm volatile("" ::: "memory");
        bool ok = true;
#pragma unroll
        for (int i = 0; i < N; ++i) u[i] = __builtin_amdgcn_raw_buffer_load_b128(rs, (int)off[i], 0, 16);
#pragma unroll
        for (int i = 0; i < N; ++i) ok &= (u[i].y == ta) & (u[i].w == tb);
        if (__all(ok)) break;
    }
}
__device__ __forceinline__ void put_unit(__amdgpu_buffer_rsrc_t rs, unsigned off, unsigned v0, unsigned v1, unsigned ta, unsigned tb) {
    v4u u; u.x = v0; u.y = ta; u.z = v1; u.w = tb;
    __builtin_amdgcn_raw_buffer_store_b128(u, rs, (int)off, 0, 16);
}
template <int n, int N>
__device__ __forceinline__ void tr_step_t(float (&v)[N], int mask, int lane) {
    const bool up = (lane & mask) != 0;
#pragma unroll
    for (int i = 0; i < n / 2; ++i) {
        const float send = up ? v[i] : v[i + n / 2];
        const float keep = up ? v[i + n / 2] : v[i];
        v[i] = keep + __shfl_xor(send, mask);
    }
}
#define tr_step(v, n, mask, lane) tr_step_t<n>(v, mask, lane)

__global__ void __launch_bounds__(NT, 2)
mla_kernel(const bf16_t* __restrict__ x, const bf16_t* __restrict__ ckv, const bf16_t* __restrict__ wq, const bf16_t* __restrict__ wuk,
           const bf16_t* __restrict__ wuv, const bf16_t* __restrict__ wo, const bf16_t* __restrict__ wg, const bf16_t* __restrict__ wu,
           const bf16_t* __restrict__ wd, const bf16_t* __restrict__ gw, bf16_t* __restrict__ out, unsigned char* __restrict__ ws) {
    extern __shared__ __attribute__((aligned(16))) unsigned char smem[];
    LAS unsigned char* L = (LAS unsigned char*)smem;
    const unsigned lds_base = (unsigned)(size_t)L;
    const int t = threadIdx.x, lane = t & 63, w = __builtin_amdgcn_readfirstlane(t >> 6);
    const int bid = blockIdx.x, xcd = bid & 7, idx = bid >> 3;
    const int h1 = xcd * 4 + (idx >> 3), j1 = idx & 7;
    const int kc = xcd * 16 + (idx >> 1), ab = kc >> 6, ai = kc & 63, hh = idx & 1;
    const int grp = idx >> 3;
    const bool has_ffn = (grp < 3) || (xcd < 4);
    const int fblk = grp < 3 ? xcd * 3 + grp : 24 + xcd;
    const int f0 = fblk * 64 + j1 * 8;
    const __amdgpu_buffer_rsrc_t rs = __builtin_amdgcn_make_buffer_rsrc((void*)ws, 0, (int)(WS_END + 65536), 0x00020000);
#define STAMP(i) do {} while (0)
    STAMP(0);
    LAS float* red = (LAS float*)(L + L_RED);
    LAS float* misc = (LAS float*)(L + L_MISC);

    const v2u ep = __builtin_amdgcn_raw_buffer_load_b64(rs, WS_EPOCH, 0, 16);
    const int s1_ch = t & 1, s1_m = t >> 1;
    const bf16_t xa0 = x[s1_m], xa1 = x[s1_m + 256], xb0 = x[512 + s1_m], xb1 = x[512 + s1_m + 256];
    const v4u wq0 = *(const v4u*)(wq + (size_t)s1_m * 4096 + h1 * 128 + 16 * j1 + 8 * s1_ch);
    const v4u wq1 = *(const v4u*)(wq + (size_t)(s1_m + 256) * 4096 + h1 * 128 + 16 * j1 + 8 * s1_ch);
    const int fr = lane & 15, fg = lane >> 4;
    v4u ukf[4];
    if (w < 4) {
#pragma unroll
        for (int ks = 0; ks < 4; ++ks) ukf[ks] = *(const v4u*)(wuk + ((size_t)(64 * j1 + 16 * w + fr) * 32 + h1) * 128 + 32 * ks + 8 * fg);
    }
    PIN();
    const bf16_t* crow = ckv + ((size_t)ab * 8192 + ai * 128) * 512;
    auto dma_rows = [&](int r0, int r1) {
#pragma unroll
        for (int r = r0; r < r1; ++r) {
            const int row = 8 * r + w;
            glds16(crow + (size_t)row * 512 + ((unsigned)lane ^ sig(row & 15)) * 8, lds_base + L_C + row * 1024);
        }
    };
    dma_rows(0, NB0);
    PIN();
    const unsigned ta = ep.x + 1u, tb = ep.y + 1u;

    {
        float v[16];
        const float a0 = bf2f(xa0), a1 = bf2f(xa1), b0 = bf2f(xb0), b1 = bf2f(xb1);
#pragma unroll
        for (int e = 0; e < 4; ++e) {
            const float l0 = bflo(wq0[e]), h0 = bfhi(wq0[e]), l1 = bflo(wq1[e]), hh1 = bfhi(wq1[e]);
            v[2 * e] = a0 * l0 + a1 * l1; v[2 * e + 1] = a0 * h0 + a1 * hh1;
            v[8 + 2 * e] = b0 * l0 + b1 * l1; v[8 + 2 * e + 1] = b0 * h0 + b1 * hh1;
        }
        tr_step(v, 16, 2, lane); tr_step(v, 8, 4, lane); tr_step(v, 4, 8, lane); tr_step(v, 2, 16, lane);
        v[0] += __shfl_xor(v[0], 32);
        const int vi = ((lane >> 1) & 1) * 8 + ((lane >> 2) & 1) * 4 + ((lane >> 3) & 1) * 2 + ((lane >> 4) & 1);
        if (lane < 32) red[w * 32 + (vi >> 3) * 16 + s1_ch * 8 + (vi & 7)] = v[0];
        BAR();
        if (t < 8) {
            float s[4] = {0.f, 0.f, 0.f, 0.f};
#pragma unroll
            for (int ww = 0; ww < 8; ++ww) {
                const f32x4 r4 = *(LAS f32x4*)(red + ww * 32 + 4 * t);
                s[0] += r4[0]; s[1] += r4[1]; s[2] += r4[2]; s[3] += r4[3];
            }
            put_unit(rs, WS_E1 + ((h1 * 2 + (t >> 2)) * 32 + 4 * j1 + (t & 3)) * 16, pk_bf16(s[0], s[1]), pk_bf16(s[2], s[3]), ta, tb);
        }
    }
    STAMP(1);
    if (w >= 4) dma_rows(NB0, NB0 + NB1);
    if (w < 4) {
        v4u bq[4];
        {
            unsigned off[8]; v4u u[8];
            const int nb = fr & 1;
#pragma unroll
            for (int ks = 0; ks < 4; ++ks) {
                off[2 * ks] = WS_E1 + ((h1 * 2 + nb) * 32 + 8 * ks + 2 * fg) * 16;
                off[2 * ks + 1] = off[2 * ks] + 16;
            }
            poll_units<8>(rs, off, u, ta, tb);
#pragma unroll
            for (int ks = 0; ks < 4; ++ks) {
                v4u b; b.x = u[2 * ks].x; b.y = u[2 * ks].z; b.z = u[2 * ks + 1].x; b.w = u[2 * ks + 1].z;
                if (fr >= 2) b = (v4u){0u, 0u, 0u, 0u};
                bq[ks] = b;
            }
        }
        f32x4 acc = {0.f, 0.f, 0.f, 0.f};
#pragma unroll
        for (int ks = 0; ks < 4; ++ks) acc = mfma16(ukf[ks], bq[ks], acc);
        const float sc = 0.08838834764831845f * 1.4426950408889634f;
        if (fr < 2) put_unit(rs, WS_E2 + ((fr * 32 + h1) * 128 + 16 * j1 + 4 * w + fg) * 16,
                             pk_bf16(acc[0] * sc, acc[1] * sc), pk_bf16(acc[2] * sc, acc[3] * sc), ta, tb);
        dma_rows(NB0, NB0 + NB1);
    }
    STAMP(2);
    dma_rows(NB0 + NB1, 16);
    PIN();
    {
        unsigned off[4]; v4u u[4];
#pragma unroll
        for (int r = 0; r < 4; ++r) off[r] = WS_E2 + ((ab * 32 + hh * 16) * 128 + t + 512 * r) * 16;
        poll_units<4>(rs, off, u, ta, tb);
#pragma unroll
        for (int r = 0; r < 4; ++r) {
            const int un = t + 512 * r, hd = un >> 7, uu = un & 127;
            v2u d; d.x = u[r].x; d.y = u[r].z;
            *(LAS v2u*)(L + L_Q + hd * 1024 + (((unsigned)(uu >> 1)) ^ sig(hd)) * 16 + (uu & 1) * 8) = d;
        }
    }
    STAMP(3);
    v4u uvw[2], wow[2], gtw, upw, wdw; bf16_t xr0, xr1, gwr;
    {
        const int q16 = t & 15, r32 = t >> 4;
        uvw[0] = *(const v4u*)(wuv + ((size_t)(64 * j1 + r32) * 32 + h1) * 128 + 8 * q16);
        uvw[1] = *(const v4u*)(wuv + ((size_t)(64 * j1 + r32 + 32) * 32 + h1) * 128 + 8 * q16);
        const int ff = has_ffn ? f0 : 0;
        gtw = *(const v4u*)(wg + (size_t)t * 1792 + ff);
        upw = *(const v4u*)(wu + (size_t)t * 1792 + ff);
        xr0 = x[t]; xr1 = x[512 + t]; gwr = gw[t];
        wow[0] = *(const v4u*)(wo + (size_t)(h1 * 128 + 16 * j1 + w) * 512 + 8 * lane);
        wow[1] = *(const v4u*)(wo + (size_t)(h1 * 128 + 16 * j1 + w + 8) * 512 + 8 * lane);
        wdw = *(const v4u*)(wd + (size_t)(ff + w) * 512 + 8 * lane);
    }
    PIN();
    asm volatile("s_waitcnt vmcnt(10)" ::: "memory");
    BAR();
    STAMP(4);
    float sv[4];
    {
        f32x4 acc = {0.f, 0.f, 0.f, 0.f};
        const unsigned sg = sig(fr);
#pragma unroll
        for (int ks = 0; ks < 16; ++ks) {
            const unsigned ch = (unsigned)(4 * ks + fg) ^ sg;
            const v4u a = *(LAS v4u*)(L + L_C + (16 * w + fr) * 1024 + ch * 16);
            const v4u b = *(LAS v4u*)(L + L_Q + fr * 1024 + ch * 16);
            acc = mfma16(a, b, acc);
        }
        sv[0] = acc[0]; sv[1] = acc[1]; sv[2] = acc[2]; sv[3] = acc[3];
    }
    float mx = fmaxf(fmaxf(sv[0], sv[1]), fmaxf(sv[2], sv[3]));
    mx = fmaxf(mx, __shfl_xor(mx, 16)); mx = fmaxf(mx, __shfl_xor(mx, 32));
    if (lane < 16) red[w * 16 + lane] = mx;
    BAR();
    float M = red[fr];
#pragma unroll
    for (int ww = 1; ww < 8; ++ww) M = fmaxf(M, red[ww * 16 + fr]);
    {
        const unsigned p01 = pk_bf16(exp2f(sv[0] - M), exp2f(sv[1] - M)), p23 = pk_bf16(exp2f(sv[2] - M), exp2f(sv[3] - M));
        float ls = (bflo(p01) + bfhi(p01)) + (bflo(p23) + bfhi(p23));
        ls += __shfl_xor(ls, 16); ls += __shfl_xor(ls, 32);
        if (lane < 16) red[128 + w * 16 + lane] = ls;
        v2u d; d.x = p01; d.y = p23;
        *(LAS v2u*)(L + L_P + fr * P_STR + (32 * (w >> 1) + 8 * fg + 4 * (w & 1)) * 2) = d;
    }
    BAR();
    {
        v4u pf[4];
#pragma unroll
        for (int ks = 0; ks < 4; ++ks) pf[ks] = *(LAS v4u*)(L + L_P + fr * P_STR + (32 * ks + 8 * fg) * 2);
        const int q = fr >> 2, p = fr & 3;
        const unsigned sg = sig(4 * fg + q);
        const int hd = hh * 16 + fr;
#pragma unroll
        for (int ct = 0; ct < 4; ++ct) {
            f32x4 acc = {0.f, 0.f, 0.f, 0.f};
            const unsigned ch = (unsigned)(8 * w + 2 * ct + (p >> 1)) ^ sg;
#pragma unroll
            for (int ks = 0; ks < 4; ++ks) {
                const v4s lo = __builtin_amdgcn_ds_read_tr16_b64_v4i16((LAS v4s*)(L + L_C + (32 * ks + 4 * fg + q) * 1024 + ch * 16 + (p & 1) * 8));
                const v4s hi = __builtin_amdgcn_ds_read_tr16_b64_v4i16((LAS v4s*)(L + L_C + (32 * ks + 16 + 4 * fg + q) * 1024 + ch * 16 + (p & 1) * 8));
                const v2u l2 = __builtin_bit_cast(v2u, lo), h2 = __builtin_bit_cast(v2u, hi);
                v4u a; a.x = l2.x; a.y = l2.y; a.z = h2.x; a.w = h2.y;
                acc = mfma16(a, pf[ks], acc);
            }
            put_unit(rs, WS_E4 + ((((hd * 8 + w) * 2 + ab) * 64 + ai) * 16 + 4 * ct + fg) * 16,
                     pk_bf16(acc[0], acc[1]), pk_bf16(acc[2], acc[3]), ta, tb);
        }
        if (t < 16) {
            float Ls = 0.f;
#pragma unroll
            for (int ww = 0; ww < 8; ++ww) Ls += red[128 + ww * 16 + t];
            put_unit(rs, WS_E4S + ((hd * 2 + ab) * 64 + ai) * 16, __float_as_uint(M), __float_as_uint(Ls), ta, tb);
        }
    }
    STAMP(5);
    {
        float v[32];
        const float x0 = bf2f(xr0), x1 = bf2f(xr1);
#pragma unroll
        for (int e = 0; e < 4; ++e) {
            const float gl = bflo(gtw[e]), gh = bfhi(gtw[e]), ul = bflo(upw[e]), uh = bfhi(upw[e]);
            v[2 * e] = x0 * gl; v[2 * e + 1] = x0 * gh; v[8 + 2 * e] = x0 * ul; v[8 + 2 * e + 1] = x0 * uh;
            v[16 + 2 * e] = x1 * gl; v[16 + 2 * e + 1] = x1 * gh; v[24 + 2 * e] = x1 * ul; v[24 + 2 * e + 1] = x1 * uh;
        }
        tr_step(v, 32, 1, lane); tr_step(v, 16, 2, lane); tr_step(v, 8, 4, lane); tr_step(v, 4, 8, lane); tr_step(v, 2, 16, lane);
        v[0] += __shfl_xor(v[0], 32);
        const int vi = (lane & 1) * 16 + ((lane >> 1) & 1) * 8 + ((lane >> 2) & 1) * 4 + ((lane >> 3) & 1) * 2 + ((lane >> 4) & 1);
        float g0 = x0 * bf2f(gwr), g1 = x1 * bf2f(gwr);
#pragma unroll
        for (int m = 1; m < 64; m <<= 1) { g0 += __shfl_xor(g0, m); g1 += __shfl_xor(g1, m); }
        if (lane < 32) red[256 + w * 40 + vi] = v[0];
        if (lane == 0) { red[256 + w * 40 + 32] = g0; red[256 + w * 40 + 33] = g1; }
        BAR();
        if (t < 16) {
            float gs = 0.f, us = 0.f;
#pragma unroll
            for (int ww = 0; ww < 8; ++ww) { gs += red[256 + ww * 40 + (t >> 3) * 16 + (t & 7)]; us += red[256 + ww * 40 + (t >> 3) * 16 + 8 + (t & 7)]; }
            const float inner = 0.7978845608028654f * (gs + 0.044715f * gs * gs * gs);
            const float th = 1.f - 2.f / (1.f + __expf(2.f * inner));
            misc[t] = has_ffn ? 0.5f * gs * (1.f + th) * us : 0.f;
        } else if (t < 18) {
            float gs = 0.f;
#pragma unroll
            for (int ww = 0; ww < 8; ++ww) gs += red[256 + ww * 40 + 32 + (t - 16)];
            misc[t] = 1.f / (1.f + __expf(-gs));
        }
    }
    {
        if (w < 2) {
            unsigned off[1]; v4u u[1];
            off[0] = WS_E4S + ((h1 * 2 + w) * 64 + lane) * 16;
            poll_units<1>(rs, off, u, ta, tb);
            const float mi = __uint_as_float(u[0].x), li = __uint_as_float(u[0].z);
            float mm = mi;
#pragma unroll
            for (int m = 1; m < 64; m <<= 1) mm = fmaxf(mm, __shfl_xor(mm, m));
            const float wi = exp2f(mi - mm);
            float ll = wi * li;
#pragma unroll
            for (int m = 1; m < 64; m <<= 1) ll += __shfl_xor(ll, m);
            misc[32 + w * 64 + lane] = wi / ll;
        }
        unsigned off[4]; v4u u[4];
        const int u16 = t & 15, r32 = t >> 4;
#pragma unroll
        for (int r = 0; r < 4; ++r) off[r] = WS_E4 + (((h1 * 8 + j1) * 128 + r32 + 32 * r) * 16 + u16) * 16;
        poll_units<4>(rs, off, u, ta, tb);
        BAR();
        float o[8];
#pragma unroll
        for (int b = 0; b < 2; ++b) {
            const float s0 = misc[32 + b * 64 + r32], s1 = misc[32 + b * 64 + r32 + 32];
            o[4 * b + 0] = s0 * bflo(u[2 * b].x) + s1 * bflo(u[2 * b + 1].x);
            o[4 * b + 1] = s0 * bfhi(u[2 * b].x) + s1 * bfhi(u[2 * b + 1].x);
            o[4 * b + 2] = s0 * bflo(u[2 * b].z) + s1 * bflo(u[2 * b + 1].z);
            o[4 * b + 3] = s0 * bfhi(u[2 * b].z) + s1 * bfhi(u[2 * b + 1].z);
        }
        tr_step(o, 8, 16, lane); tr_step(o, 4, 32, lane);
        LAS float* r2 = (LAS float*)(L + L_C);
        {
            const int b = (lane >> 4) & 1, c = 4 * u16 + 2 * ((lane >> 5) & 1);
            f32x2 d; d[0] = o[0]; d[1] = o[1];
            *(LAS f32x2*)(r2 + (w * 2 + b) * 64 + c) = d;
        }
        BAR();
        if (t < 128) {
            float s = 0.f;
#pragma unroll
            for (int ww = 0; ww < 8; ++ww) s += r2[ww * 128 + t];
            misc[192 + t] = s;
        }
        BAR();
    }
    STAMP(6);
    {
        const int q16 = t & 15, r32 = t >> 4;
        float v[16];
        const float o00 = misc[192 + r32], o01 = misc[192 + r32 + 32], o10 = misc[256 + r32], o11 = misc[256 + r32 + 32];
#pragma unroll
        for (int e = 0; e < 4; ++e) {
            const float l0 = bflo(uvw[0][e]), h0 = bfhi(uvw[0][e]), l1 = bflo(uvw[1][e]), h1v = bfhi(uvw[1][e]);
            v[2 * e] = o00 * l0 + o01 * l1; v[2 * e + 1] = o00 * h0 + o01 * h1v;
            v[8 + 2 * e] = o10 * l0 + o11 * l1; v[8 + 2 * e + 1] = o10 * h0 + o11 * h1v;
        }
        tr_step(v, 16, 16, lane); tr_step(v, 8, 32, lane);
        LAS float* r3 = (LAS float*)(L + L_C + 8192);
        {
            const int b = (lane >> 4) & 1, d = 8 * q16 + 4 * ((lane >> 5) & 1);
            f32x4 dd; dd[0] = v[0]; dd[1] = v[1]; dd[2] = v[2]; dd[3] = v[3];
            *(LAS f32x4*)(r3 + (w * 2 + b) * 128 + d) = dd;
        }
        BAR();
        if (t < 128) {
            float s0 = 0.f, s1 = 0.f;
#pragma unroll
            for (int ww = 0; ww < 8; ++ww) { const f32x2 r = *(LAS f32x2*)(r3 + ww * 256 + 2 * t); s0 += r[0]; s1 += r[1]; }
            const int b = t >> 6, dp = t & 63, jd = dp >> 3;
            put_unit(rs, WS_E5 + ((((h1 * 8 + jd) * 8 + j1) * 2 + b) * 8 + (dp & 7)) * 16, __float_as_uint(s0), __float_as_uint(s1), ta, tb);
        }
    }
    STAMP(7);
    {
        if (w < 2) {
            unsigned off[1]; v4u u[1];
            off[0] = WS_E5 + ((h1 * 8 + j1) * 128 + t) * 16;
            poll_units<1>(rs, off, u, ta, tb);
            float c0 = __uint_as_float(u[0].x), c1 = __uint_as_float(u[0].z);
            c0 += __shfl_xor(c0, 16); c1 += __shfl_xor(c1, 16); c0 += __shfl_xor(c0, 32); c1 += __shfl_xor(c1, 32);
            if (lane < 16) { misc[320 + w * 32 + 2 * lane] = c0; misc[320 + w * 32 + 2 * lane + 1] = c1; }
        }
        BAR();
    }
    STAMP(8);
    {
        const float g0 = misc[16], g1 = misc[17];
        const float c00 = g0 * (misc[320 + w] + misc[352 + w]), c01 = g0 * (misc[320 + w + 8] + misc[352 + w + 8]);
        const float c10 = g1 * (misc[336 + w] + misc[368 + w]), c11 = g1 * (misc[336 + w + 8] + misc[368 + w + 8]);
        const float f0v = (1.f - g0) * misc[w], f1v = (1.f - g1) * misc[8 + w];
        float v[16];
#pragma unroll
        for (int e = 0; e < 4; ++e) {
            const float l0 = bflo(wow[0][e]), h0 = bfhi(wow[0][e]), l1 = bflo(wow[1][e]), h1v = bfhi(wow[1][e]), ld = bflo(wdw[e]), hd = bfhi(wdw[e]);
            v[2 * e] = c00 * l0 + c01 * l1 + f0v * ld; v[2 * e + 1] = c00 * h0 + c01 * h1v + f0v * hd;
            v[8 + 2 * e] = c10 * l0 + c11 * l1 + f1v * ld; v[8 + 2 * e + 1] = c10 * h0 + c11 * h1v + f1v * hd;
        }
        LAS float* r4 = (LAS float*)(L + L_C + 16384);
#pragma unroll
        for (int b = 0; b < 2; ++b) {
            f32x4 d0, d1;
            d0[0] = v[8 * b]; d0[1] = v[8 * b + 1]; d0[2] = v[8 * b + 2]; d0[3] = v[8 * b + 3];
            d1[0] = v[8 * b + 4]; d1[1] = v[8 * b + 5]; d1[2] = v[8 * b + 6]; d1[3] = v[8 * b + 7];
            *(LAS f32x4*)(r4 + (w * 2 + b) * 512 + 8 * lane) = d0;
            *(LAS f32x4*)(r4 + (w * 2 + b) * 512 + 8 * lane + 4) = d1;
        }
        BAR();
        float s0 = 0.f, s1 = 0.f;
#pragma unroll
        for (int ww = 0; ww < 8; ++ww) { const f32x2 r = *(LAS f32x2*)(r4 + ww * 1024 + 2 * t); s0 += r[0]; s1 += r[1]; }
        put_unit(rs, WS_E6 + (bid * 512 + t) * 16, __float_as_uint(s0), __float_as_uint(s1), ta, tb);
    }
    STAMP(9);
    {
        unsigned off[1]; v4u u[1];
        off[0] = WS_E6 + ((t >> 1) * 512 + 2 * bid + (t & 1)) * 16;
        poll_units<1>(rs, off, u, ta, tb);
        float c0 = __uint_as_float(u[0].x), c1 = __uint_as_float(u[0].z);
#pragma unroll
        for (int m = 2; m < 64; m <<= 1) { c0 += __shfl_xor(c0, m); c1 += __shfl_xor(c1, m); }
        if (lane < 2) { red[w * 4 + 2 * lane] = c0; red[w * 4 + 2 * lane + 1] = c1; }
        BAR();
        if (t == 0) {
            f32x4 s = {0.f, 0.f, 0.f, 0.f};
#pragma unroll
            for (int ww = 0; ww < 8; ++ww) s += *(LAS f32x4*)(red + ww * 4);
            v2u o; o.x = pk_bf16(s[0], s[1]); o.y = pk_bf16(s[2], s[3]);
            *(v2u*)(out + 4 * bid) = o;
            if (bid == 0) { v2u e2; e2.x = ta; e2.y = tb; __builtin_amdgcn_raw_buffer_store_b64(e2, rs, WS_EPOCH, 0, 16); }
        }
    }
    STAMP(10);
}

extern "C" void kernel_launch(void* const* d_in, const int* in_sizes, int n_in, void* d_out, int out_size, void* d_ws, size_t ws_size, hipStream_t stream) {
    static bool attr_set = false;
    if (!attr_set) { (void)hipFuncSetAttribute((const void*)mla_kernel, hipFuncAttributeMaxDynamicSharedMemorySize, L_TOTAL); attr_set = true; }
    mla_kernel<<<NWG, NT, L_TOTAL, stream>>>((const bf16_t*)d_in[0], (const bf16_t*)d_in[1], (const bf16_t*)d_in[2], (const bf16_t*)d_in[3],
        (const bf16_t*)d_in[4], (const bf16_t*)d_in[5], (const bf16_t*)d_in[7], (const bf16_t*)d_in[8], (const bf16_t*)d_in[9], (const bf16_t*)d_in[10],
        (bf16_t*)d_out, (unsigned char*)d_ws);
}
```

```cpp
#include <hip/hip_runtime.h>
#include <stdint.h>

#define GAS __attribute__((address_space(1)))
#define LAS __attribute__((address_space(3)))
typedef unsigned short bf16_t;
typedef unsigned v4u __attribute__((ext_vector_type(4)));
typedef unsigned v2u __attribute__((ext_vector_type(2)));
typedef float f32x4 __attribute__((ext_vector_type(4)));
typedef float f32x2 __attribute__((ext_vector_type(2)));
typedef short bf16x8 __attribute__((ext_vector_type(8)));
typedef short v4s __attribute__((ext_vector_type(4)));
typedef __bf16 bf2v __attribute__((ext_vector_type(2)));

#define STAMPS 0
#define PSLEEP 2
#define ASLEEP 2
#define CDELAY 150
#define TCLR 250
#define NCA 8
#define ADELAY 150
#define WDELAY 450
#define CODEPF 1
#define CODE_SPAN 16384

constexpr int NWG = 256, NT = 512;
constexpr unsigned WS_EPOCH = 0;
constexpr unsigned WS_E1 = 4096;
constexpr unsigned WS_E2 = WS_E1 + 131072;
constexpr unsigned WS_CLR = WS_E2 + 65536;
constexpr unsigned WS_E4S = WS_E2 + 131072;
constexpr unsigned WS_E7 = WS_E4S + 65536;
constexpr unsigned WS_E6 = WS_E7 + 262144;
constexpr unsigned WS_E4 = WS_E6 + 1048576;
constexpr unsigned WS_END = WS_E4 + 4194304;
constexpr unsigned WS_STAMP = WS_END;
constexpr unsigned SENT = 0xFFFFFFFFu;

constexpr int L_C = 0, L_Q = 131072, L_TOTAL = 163840, P_STR = 272;
constexpr int SPIN_CAP = 20000;

__device__ __forceinline__ float bflo(unsigned u) { return __uint_as_float(u << 16); }
__device__ __forceinline__ float bfhi(unsigned u) { return __uint_as_float(u & 0xffff0000u); }
__device__ __forceinline__ float bf2f(bf16_t v) { return __uint_as_float(((unsigned)v) << 16); }
__device__ __forceinline__ unsigned pk_bf16(float lo, float hi) { bf2v r; r[0] = (__bf16)lo; r[1] = (__bf16)hi; return __builtin_bit_cast(unsigned, r); }
__device__ __forceinline__ unsigned pk_ns(float lo, float hi) { const unsigned r = pk_bf16(lo, hi); return r == SENT ? 0x7FC07FC0u : r; }
__device__ __forceinline__ float dot2(unsigned a, unsigned b, float c) { return __builtin_amdgcn_fdot2_f32_bf16(__builtin_bit_cast(bf2v, a), __builtin_bit_cast(bf2v, b), c, false); }
__device__ __forceinline__ unsigned sig(unsigned r) { return ((r & 3u) << 1) | ((((r >> 2) ^ (r >> 3)) & 1u) << 3) | (r >> 3); }
__device__ __forceinline__ f32x4 mfma16(v4u a, v4u b, f32x4 c) {
    return __builtin_amdgcn_mfma_f32_16x16x32_bf16(__builtin_bit_cast(bf16x8, a), __builtin_bit_cast(bf16x8, b), c, 0, 0, 0);
}
__device__ __forceinline__ void glds16(const void* gsrc, unsigned lds_dst) {
    unsigned keep;
    asm volatile("s_mov_b32 %0, m0\n\ts_mov_b32 m0, %2\n\ts_nop 0\n\tglobal_load_lds_dwordx4 %1, off nt\n\ts_mov_b32 m0, %0"
                 : "=&s"(keep) : "v"(gsrc), "s"(lds_dst) : "memory");
}
__device__ __forceinline__ void glds16_plain(const void* gsrc, unsigned lds_dst) {
    unsigned keep;
    asm volatile("s_mov_b32 %0, m0\n\ts_mov_b32 m0, %2\n\ts_nop 0\n\tglobal_load_lds_dwordx4 %1, off\n\ts_mov_b32 m0, %0"
                 : "=&s"(keep) : "v"(gsrc), "s"(lds_dst) : "memory");
}
__device__ __forceinline__ v4u ldnt_asm(const void* p) { v4u r; asm volatile("global_load_dwordx4 %0, %1, off nt" : "=v"(r) : "v"(p) : "memory"); return r; }
#define LDNT(p) __builtin_nontemporal_load((const v4u*)(p))
#define BAR() do { asm volatile("s_waitcnt lgkmcnt(0)" ::: "memory"); __builtin_amdgcn_s_barrier(); asm volatile("" ::: "memory"); } while (0)
#define PIN() do { asm volatile("" ::: "memory"); __builtin_amdgcn_sched_barrier(0); } while (0)
#define LWAIT() asm volatile("s_waitcnt lgkmcnt(0)" ::: "memory")
#define VWAIT0() asm volatile("s_waitcnt vmcnt(0)" ::: "memory")
__device__ __forceinline__ float red32(float a, float b) {
    auto r = __builtin_amdgcn_permlane32_swap(__float_as_uint(a), __float_as_uint(b), false, false);
    return __uint_as_float(r[0]) + __uint_as_float(r[1]);
}
__device__ __forceinline__ float red16(float a, float b) {
    auto r = __builtin_amdgcn_permlane16_swap(__float_as_uint(a), __float_as_uint(b), false, false);
    return __uint_as_float(r[0]) + __uint_as_float(r[1]);
}
template <int CTRL> __device__ __forceinline__ float dpp(float v) { return __uint_as_float(__builtin_amdgcn_update_dpp(0, __float_as_uint(v), CTRL, 0xf, 0xf, true)); }
#define QX1 0xB1
#define QX2 0x4E
#define ROR4 0x124
#define ROR8 0x128
__device__ __forceinline__ float max_hi(float v) {
    auto r = __builtin_amdgcn_permlane16_swap(__float_as_uint(v), __float_as_uint(v), false, false);
    v = fmaxf(__uint_as_float(r[0]), __uint_as_float(r[1]));
    auto s = __builtin_amdgcn_permlane32_swap(__float_as_uint(v), __float_as_uint(v), false, false);
    return fmaxf(__uint_as_float(s[0]), __uint_as_float(s[1]));
}
__device__ __forceinline__ float sum_hi(float v) { v = red16(v, v); return red32(v, v); }
__device__ __forceinline__ float max_all(float v) {
    v = fmaxf(v, dpp<QX1>(v)); v = fmaxf(v, dpp<QX2>(v)); v = fmaxf(v, dpp<ROR4>(v)); v = fmaxf(v, dpp<ROR8>(v)); return max_hi(v);
}
__device__ __forceinline__ float sum_all(float v) {
    v += dpp<QX1>(v); v += dpp<QX2>(v); v += dpp<ROR4>(v); v += dpp<ROR8>(v); return sum_hi(v);
}
__device__ __forceinline__ v4u tr_frag(LAS unsigned char* p0, LAS unsigned char* p1) {
    const v2u l2 = __builtin_bit_cast(v2u, __builtin_amdgcn_ds_read_tr16_b64_v4i16((LAS v4s*)p0));
    const v2u h2 = __builtin_bit_cast(v2u, __builtin_amdgcn_ds_read_tr16_b64_v4i16((LAS v4s*)p1));
    v4u a; a.x = l2.x; a.y = l2.y; a.z = h2.x; a.w = h2.y; return a;
}
__device__ __forceinline__ v4u ld_sc1(__amdgpu_buffer_rsrc_t rs, unsigned off) { asm volatile("" : "+v"(off)); return __builtin_amdgcn_raw_buffer_load_b128(rs, (int)off, 0, 16); }
template <int N, int SLP = PSLEEP>
__device__ __forceinline__ void poll_units(__amdgpu_buffer_rsrc_t rs, const unsigned (&off)[N], v4u (&u)[N], unsigned ta, unsigned tb) {
#pragma unroll
    for (int i = 0; i < N; ++i) u[i] = ld_sc1(rs, off[i]);
#pragma unroll 1
    for (int spin = 0; spin < SPIN_CAP; ++spin) {
        bool ok = true;
#pragma unroll
        for (int i = 0; i < N; ++i) ok &= (u[i].y == ta) & (u[i].w == tb);
        if (__all(ok)) break;
        __builtin_amdgcn_s_sleep(SLP);
        asm volatile("" ::: "memory");
        if (!ok) {
#pragma unroll
            for (int i = 0; i < N; ++i) u[i] = ld_sc1(rs, off[i]);
        }
    }
}
__device__ __forceinline__ void put_unit(__amdgpu_buffer_rsrc_t rs, unsigned off, unsigned v0, unsigned v1, unsigned ta, unsigned tb) {
    v4u u; u.x = v0; u.y = ta; u.z = v1; u.w = tb;
    __builtin_amdgcn_raw_buffer_store_b128(u, rs, (int)off, 0, 16);
}

__global__ void __launch_bounds__(NT, 2)
mla_kernel(const bf16_t* __restrict__ x, const bf16_t* __restrict__ ckv, const bf16_t* __restrict__ wq, const bf16_t* __restrict__ wuk,
           const bf16_t* __restrict__ wuv, const bf16_t* __restrict__ wo, const bf16_t* __restrict__ wg, const bf16_t* __restrict__ wu,
           const bf16_t* __restrict__ wd, const bf16_t* __restrict__ gw, bf16_t* out, unsigned char* ws) {
    extern __shared__ __attribute__((aligned(16))) unsigned char smem[];
    LAS unsigned char* L = (LAS unsigned char*)smem;
    const unsigned lds_base = (unsigned)(size_t)L;
    const int t = threadIdx.x, lane = t & 63, w = __builtin_amdgcn_readfirstlane(t >> 6);
    const int bid = blockIdx.x, xcd = bid & 7, idx = bid >> 3;
    const unsigned long long t_start = __builtin_amdgcn_s_memrealtime();
    {
        unsigned long long pc;
        asm volatile("s_getpc_b64 %0" : "=s"(pc));
        const int sl = (idx & 15) * 8 + w;
        if (idx >= 16 && (sl + 1) * 1024 <= CODE_SPAN) glds16_plain((const void*)(pc + (unsigned)sl * 1024u + lane * 16u), lds_base + L_Q + w * 1024);
    }
    asm volatile("" :: "s"(x), "s"(ckv), "s"(wq), "s"(wuk), "s"(wuv), "s"(wo), "s"(wg), "s"(wu), "s"(wd), "s"(gw), "s"(out), "s"(ws));
    const __amdgpu_buffer_rsrc_t rs = __builtin_amdgcn_make_buffer_rsrc((void*)ws, 0, (int)(WS_END + 65536), 0x00020000);
#define STAMP(i) do {} while (0)
    const int fr = lane & 15, fg = lane >> 4;
    const int q16 = t & 15, r32 = t >> 4;
    const int tq = fr >> 2, tp = fr & 3;
    const unsigned sgt = sig(4 * fg + tq);

    if (__builtin_expect(idx < 16, 1)) {
        const int h1 = xcd * 4 + (idx >> 2), j1 = idx & 3, wn = xcd * 16 + idx;
        LAS float* r1 = (LAS float*)(L + L_Q);
        LAS bf16_t* oimg = (LAS bf16_t*)(L + L_Q + 8704);
        LAS unsigned char* cimg = L + L_Q + 9728;
        LAS float* fin = (LAS float*)(L + L_Q + 11008);
        LAS unsigned char* qimg = L + L_Q + 12288;
        constexpr int L_WD = L_Q + 16384;
        constexpr int L_UV = L_C + 98304;
        const v2u ep = __builtin_amdgcn_raw_buffer_load_b64(rs, WS_EPOCH, 0, 0);
        if (w == 1 && lane < 32) {
            const v4u sv = {SENT, SENT, SENT, SENT};
            __builtin_amdgcn_raw_buffer_store_b128(sv, rs, (int)(WS_E2 + ((lane >> 4) * 32 + h1) * 1024 + 256 * j1 + (lane & 15) * 16), 0, 16);
        }
        PIN();
        const bf16_t* wqp = wq + (size_t)(128 * j1 + 4 * r32) * 4096 + h1 * 128 + 8 * q16;
        const v4u wq0 = LDNT(wqp), wq1 = LDNT(wqp + 4096), wq2 = LDNT(wqp + 8192), wq3 = LDNT(wqp + 12288);
        const v2u xa = *(const v2u*)(x + 128 * j1 + 4 * r32), xb = *(const v2u*)(x + 512 + 128 * j1 + 4 * r32);
        PIN();
        __builtin_amdgcn_s_barrier();
        const unsigned ta = ep.x + 1u, tb = ep.y + 1u;
        if (w == 1) {
            asm volatile("s_waitcnt vmcnt(6)" ::: "memory");
            if (lane == 0) put_unit(rs, WS_CLR + wn * 16, 0u, 0u, ta, tb);
        }
        PIN();
        STAMP(5);
        v4u ukf[4];
#pragma unroll
        for (int ks = 0; ks < 4; ++ks) ukf[ks] = LDNT(wuk + ((size_t)(128 * j1 + 16 * w + fr) * 32 + h1) * 128 + 32 * ks + 8 * fg);
        const bf16_t xg0 = x[t], xg1 = x[512 + t], gwr = gw[t];
        PIN();
        {
            float a[16];
#pragma unroll
            for (int e = 0; e < 4; ++e) {
                const unsigned lo = __builtin_amdgcn_perm(wq1[e], wq0[e], 0x05040100u), hi = __builtin_amdgcn_perm(wq1[e], wq0[e], 0x07060302u);
                const unsigned lo2 = __builtin_amdgcn_perm(wq3[e], wq2[e], 0x05040100u), hi2 = __builtin_amdgcn_perm(wq3[e], wq2[e], 0x07060302u);
                a[2 * e] = dot2(lo2, xa.y, dot2(lo, xa.x, 0.f)); a[2 * e + 1] = dot2(hi2, xa.y, dot2(hi, xa.x, 0.f));
                a[8 + 2 * e] = dot2(lo2, xb.y, dot2(lo, xb.x, 0.f)); a[8 + 2 * e + 1] = dot2(hi2, xb.y, dot2(hi, xb.x, 0.f));
            }
            float r8[8], r4[4];
#pragma unroll
            for (int i = 0; i < 8; ++i) r8[i] = red32(a[i], a[8 + i]);
#pragma unroll
            for (int i = 0; i < 4; ++i) r4[i] = red16(r8[i], r8[4 + i]);
            const int b = lane >> 5, d = 8 * q16 + 4 * ((lane >> 4) & 1);
            f32x4 dd; dd[0] = r4[0]; dd[1] = r4[1]; dd[2] = r4[2]; dd[3] = r4[3];
            *(LAS f32x4*)(r1 + (w * 2 + b) * 128 + d) = dd;
        }
        STAMP(7);
        BAR();
        if (w == 0) {
            f32x4 s = {0.f, 0.f, 0.f, 0.f};
#pragma unroll
            for (int ww = 0; ww < 8; ++ww) s += *(LAS f32x4*)(r1 + ww * 256 + 4 * lane);
            put_unit(rs, WS_E1 + ((h1 * 4 + j1) * 64 + lane) * 16, pk_bf16(s[0], s[1]), pk_bf16(s[2], s[3]), ta, tb);
            STAMP(1);
            unsigned off[4]; v4u u[4];
#pragma unroll
            for (int r = 0; r < 4; ++r) off[r] = WS_E1 + ((h1 * 4 + r) * 64 + lane) * 16;
            poll_units<4>(rs, off, u, ta, tb);
#pragma unroll
            for (int r = 0; r < 4; ++r) { v2u d; d.x = u[r].x; d.y = u[r].z; *(LAS v2u*)(qimg + (2 * r + (lane >> 5)) * 272 + (lane & 31) * 8) = d; }
        }
        const float gg0 = sum_all(bf2f(xg0) * bf2f(gwr)), gg1 = sum_all(bf2f(xg1) * bf2f(gwr));
        BAR();
        {
            f32x4 acc = {0.f, 0.f, 0.f, 0.f};
#pragma unroll
            for (int ks = 0; ks < 4; ++ks) {
                const v4u b = *(LAS v4u*)(qimg + (fr & 7) * 272 + (32 * ks + 8 * fg) * 2);
                acc = mfma16(ukf[ks], b, acc);
            }
            const float sc = 0.08838834764831845f * 1.4426950408889634f;
            float qa[4];
#pragma unroll
            for (int e = 0; e < 4; ++e) { float v = acc[e]; v += dpp<QX2>(v); v += dpp<ROR4>(v); qa[e] = v * sc; }
            if (fr < 2) { v2u d; d.x = pk_ns(qa[0], qa[1]); d.y = pk_ns(qa[2], qa[3]);
                __builtin_amdgcn_raw_buffer_store_b64(d, rs, (int)(WS_E2 + (fr * 32 + h1) * 1024 + (128 * j1 + 16 * w + 4 * fg) * 2), 0, 16); }
        }
        STAMP(2);
        while (__builtin_amdgcn_s_memrealtime() - t_start < (unsigned long long)WDELAY) __builtin_amdgcn_s_sleep(2);
        VWAIT0();
        const bool has = wn < 112;
        const int cb = has ? (wn >> 2) : 0, f0 = 64 * cb + 16 * j1;
        const bf16_t* gp = ((q16 & 8) ? wu : wg) + (size_t)(128 * j1 + 4 * r32) * 1792 + 64 * cb + 8 * (q16 & 7);
        v4u g0 = ldnt_asm(gp), g1 = ldnt_asm(gp + 1792), g2 = ldnt_asm(gp + 2 * 1792), g3 = ldnt_asm(gp + 3 * 1792);
#pragma unroll
        for (int r = 0; r < 4; ++r) {
            const int row = 16 * w + 4 * r + (lane >> 4);
            glds16(wuv + ((size_t)(128 * j1 + row) * 32 + h1) * 128 + ((unsigned)(lane & 15) ^ sig(row & 15)) * 8, lds_base + L_UV + (16 * w + 4 * r) * 256);
        }
        glds16(wd + (size_t)(f0 + 2 * w) * 512 + ((unsigned)lane ^ sig(2 * w)) * 8, lds_base + L_WD + (2 * w) * 1024);
        glds16(wd + (size_t)(f0 + 2 * w + 1) * 512 + ((unsigned)lane ^ sig(2 * w + 1)) * 8, lds_base + L_WD + (2 * w + 1) * 1024);
        const bf16_t* wop = wo + (size_t)(h1 * 128) * 512;
#define WODMA(row) glds16(wop + (size_t)(row) * 512 + ((unsigned)lane ^ sig((row) & 15)) * 8, lds_base + L_C + (row) * 1024)
#pragma unroll
        for (int r = 0; r < 6; ++r) { int rr = r + 3 * j1; rr = rr >= 12 ? rr - 12 : rr; WODMA(8 * rr + w); }
        asm volatile("s_waitcnt vmcnt(12)" : "+v"(g0), "+v"(g1), "+v"(g2), "+v"(g3) :: "memory");
        {
            float a[16];
#pragma unroll
            for (int e = 0; e < 4; ++e) {
                const unsigned lo = __builtin_amdgcn_perm(g1[e], g0[e], 0x05040100u), hi = __builtin_amdgcn_perm(g1[e], g0[e], 0x07060302u);
                const unsigned lo2 = __builtin_amdgcn_perm(g3[e], g2[e], 0x05040100u), hi2 = __builtin_amdgcn_perm(g3[e], g2[e], 0x07060302u);
                a[2 * e] = dot2(lo2, xa.y, dot2(lo, xa.x, 0.f)); a[2 * e + 1] = dot2(hi2, xa.y, dot2(hi, xa.x, 0.f));
                a[8 + 2 * e] = dot2(lo2, xb.y, dot2(lo, xb.x, 0.f)); a[8 + 2 * e + 1] = dot2(hi2, xb.y, dot2(hi, xb.x, 0.f));
            }
            float r8[8], r4[4];
#pragma unroll
            for (int i = 0; i < 8; ++i) r8[i] = red32(a[i], a[8 + i]);
#pragma unroll
            for (int i = 0; i < 4; ++i) r4[i] = red16(r8[i], r8[4 + i]);
            const int b = lane >> 5, d = 8 * q16 + 4 * ((lane >> 4) & 1);
            f32x4 dd; dd[0] = r4[0]; dd[1] = r4[1]; dd[2] = r4[2]; dd[3] = r4[3];
            *(LAS f32x4*)(r1 + (w * 2 + b) * 128 + d) = dd;
            LAS float* gts = (LAS float*)(L + L_Q + 8192);
            if (lane == 0) { gts[2 * w] = gg0; gts[2 * w + 1] = gg1; }
        }
        asm volatile("s_waitcnt vmcnt(8)" ::: "memory");
        BAR();
        if (w < 2) {
            f32x2 s = {0.f, 0.f};
#pragma unroll
            for (int ww = 0; ww < 8; ++ww) s += *(LAS f32x2*)(r1 + ww * 256 + 2 * t);
            put_unit(rs, WS_E7 + (((wn >> 2) * 4 + j1) * 128 + t) * 16, __float_as_uint(s[0]), __float_as_uint(s[1]), ta, tb);
        }
        float ga;
        {
            LAS float* gts = (LAS float*)(L + L_Q + 8192);
            float s = 0.f;
#pragma unroll
            for (int ww = 0; ww < 8; ++ww) s += gts[2 * ww + (fr & 1)];
            ga = 1.f / (1.f + __expf(-s));
        }
        v4u uvf[4];
        {
            const unsigned ch = (unsigned)(2 * w + (tp >> 1)) ^ sgt;
            LAS unsigned char* base = L + L_UV + (4 * fg + tq) * 256 + ch * 16 + (tp & 1) * 8;
#pragma unroll
            for (int ks = 0; ks < 4; ++ks) uvf[ks] = tr_frag(base + 32 * ks * 256, base + (32 * ks + 16) * 256);
        }
        BAR();
#pragma unroll
        for (int r = 6; r < 12; ++r) { int rr = r + 3 * j1; rr = rr >= 12 ? rr - 12 : rr; WODMA(8 * rr + w); }
#pragma unroll
        for (int r = 12; r < 16; ++r) WODMA(8 * r + w);
        if (w == 0) {
            const int kq2 = lane >> 5, bb = (lane >> 4) & 1, mat = (lane >> 3) & 1, cp = lane & 7;
            unsigned off[2]; v4u u[2];
            off[0] = WS_E7 + (((wn >> 2) * 4 + kq2) * 128 + bb * 64 + mat * 32 + 8 * j1 + cp) * 16; off[1] = off[0] + 2 * 128 * 16;
            poll_units<2>(rs, off, u, ta, tb);
            float v0 = __uint_as_float(u[0].x) + __uint_as_float(u[1].x), v1 = __uint_as_float(u[0].z) + __uint_as_float(u[1].z);
            v0 = red32(v0, v0); v1 = red32(v1, v1);
            const float o0 = dpp<ROR8>(v0), o1 = dpp<ROR8>(v1);
            const float gs0 = mat ? o0 : v0, us0 = mat ? v0 : o0, gs1 = mat ? o1 : v1, us1 = mat ? v1 : o1;
            LAS float* gts = (LAS float*)(L + L_Q + 8192);
            float sg = 0.f;
#pragma unroll
            for (int ww = 0; ww < 8; ++ww) sg += gts[2 * ww + bb];
            const float og = 1.f - 1.f / (1.f + __expf(-sg));
            const float th0 = 1.f - 2.f / (1.f + __expf(1.5957691216057308f * (gs0 + 0.044715f * gs0 * gs0 * gs0)));
            const float th1 = 1.f - 2.f / (1.f + __expf(1.5957691216057308f * (gs1 + 0.044715f * gs1 * gs1 * gs1)));
            const float h0 = has ? 0.5f * gs0 * (1.f + th0) * us0 * og : 0.f, h1v = has ? 0.5f * gs1 * (1.f + th1) * us1 * og : 0.f;
            const unsigned hp = pk_bf16(h0, h1v);
            const unsigned val = kq2 ? pk_bf16(h0 - bflo(hp), h1v - bfhi(hp)) : hp;
            LAS unsigned* ci = (LAS unsigned*)(cimg + ((kq2 * 2 + bb) * 160 + 128 + 8 * (cp >> 1) + 2 * (cp & 1) + 4 * mat) * 2);
            *ci = mat ? 0u : val;
        }
        VWAIT0();
        BAR();
        v4u wof[4][4];
        f32x4 oacc[4];
        {
            const v4u cf4 = *(LAS v4u*)(cimg + ((fr & 3) * 160 + 128 + 8 * fg) * 2);
#pragma unroll
            for (int ct = 0; ct < 4; ++ct) {
                const unsigned ch = (unsigned)(8 * w + 2 * ct + (tp >> 1)) ^ sgt;
                LAS unsigned char* base = L + L_C + (4 * fg + tq) * 1024 + ch * 16 + (tp & 1) * 8;
#pragma unroll
                for (int ks = 0; ks < 4; ++ks) wof[ct][ks] = tr_frag(base + 32 * ks * 1024, base + (32 * ks + 16) * 1024);
                oacc[ct] = mfma16(tr_frag(base + (L_WD - L_C), base + (L_WD - L_C)), cf4, (f32x4){0.f, 0.f, 0.f, 0.f});
            }
        }
        STAMP(8);
        {
            v4u u[4], st[2];
            const unsigned o0 = WS_E4 + ((((h1 >> 1) * 4 + j1) * 128 + r32) * 32 + (q16 >> 2) * 8 + (h1 & 1) * 4 + (q16 & 3)) * 16;
            {
                const unsigned s0o = WS_E4S + ((h1 * 2) * 64 + lane) * 16;
                unsigned off[2] = {s0o, s0o + 1024};
                if (w == 0) poll_units<2>(rs, off, st, ta, tb);
                STAMP(3);
                BAR();
                if (w != 0) poll_units<2>(rs, off, st, ta, tb);
#pragma unroll
                for (int r = 0; r < 4; ++r) u[r] = ld_sc1(rs, o0 + r * 32 * 512);
            }
            float sc[2];
#pragma unroll
            for (int b = 0; b < 2; ++b) {
                const float mi = __uint_as_float(st[b].x), li = __uint_as_float(st[b].z);
                const float wi = exp2f(mi - max_all(mi));
                sc[b] = wi / sum_all(wi * li);
            }
            const float s00 = __shfl(sc[0], r32), s01 = __shfl(sc[0], r32 + 32), s10 = __shfl(sc[1], r32), s11 = __shfl(sc[1], r32 + 32);
#pragma unroll 1
            for (int spin = 0; spin < SPIN_CAP; ++spin) {
                bool ok = true;
#pragma unroll
                for (int r = 0; r < 4; ++r) ok &= (u[r].x != SENT) & (u[r].y != SENT) & (u[r].z != SENT) & (u[r].w != SENT);
                if (__all(ok)) break;
                __builtin_amdgcn_s_sleep(1);
                asm volatile("" ::: "memory");
                if (!ok) {
#pragma unroll
                    for (int r = 0; r < 4; ++r) u[r] = ld_sc1(rs, o0 + r * 32 * 512);
                }
            }
            STAMP(4);
            float o[16];
#pragma unroll
            for (int e = 0; e < 4; ++e) {
                o[2 * e] = s00 * bflo(u[0][e]) + s01 * bflo(u[1][e]); o[2 * e + 1] = s00 * bfhi(u[0][e]) + s01 * bfhi(u[1][e]);
                o[8 + 2 * e] = s10 * bflo(u[2][e]) + s11 * bflo(u[3][e]); o[8 + 2 * e + 1] = s10 * bfhi(u[2][e]) + s11 * bfhi(u[3][e]);
            }
            float r8[8], r4[4];
#pragma unroll
            for (int i = 0; i < 8; ++i) r8[i] = red32(o[i], o[8 + i]);
#pragma unroll
            for (int i = 0; i < 4; ++i) r4[i] = red16(r8[i], r8[4 + i]);
            const int c = 64 * (q16 >> 3) + 16 * (2 * ((q16 >> 2) & 1) + ((lane >> 4) & 1)) + 4 * (q16 & 3);
            f32x4 dd; dd[0] = r4[0]; dd[1] = r4[1]; dd[2] = r4[2]; dd[3] = r4[3];
            *(LAS f32x4*)(r1 + (w * 2 + (lane >> 5)) * 128 + c) = dd;
        }
        BAR();
        STAMP(9);
        {
            f32x4 s = {0.f, 0.f, 0.f, 0.f};
#pragma unroll
            for (int ww = 0; ww < 8; ++ww) s += *(LAS f32x4*)(r1 + ww * 256 + 4 * lane);
            v2u hp, lp;
            hp.x = pk_bf16(s[0], s[1]); hp.y = pk_bf16(s[2], s[3]);
            lp.x = pk_bf16(s[0] - bflo(hp.x), s[1] - bfhi(hp.x)); lp.y = pk_bf16(s[2] - bflo(hp.y), s[3] - bfhi(hp.y));
            const int c = 4 * (lane & 31), bb = lane >> 5;
            const int pos = 32 * (c >> 5) + 8 * ((c >> 2) & 3) + 4 * ((c >> 4) & 1);
            *(LAS v2u*)(oimg + bb * 128 + pos) = hp;
            *(LAS v2u*)(oimg + (2 + bb) * 128 + pos) = lp;
            LWAIT();
            f32x4 acc = {0.f, 0.f, 0.f, 0.f};
#pragma unroll
            for (int ks = 0; ks < 4; ++ks) {
                const v4u b = *(LAS v4u*)(oimg + (fr & 3) * 128 + 32 * ks + 8 * fg);
                acc = mfma16(uvf[ks], b, acc);
            }
            float cx[4];
#pragma unroll
            for (int e = 0; e < 4; ++e) cx[e] = (acc[e] + dpp<QX2>(acc[e])) * ga;
            unsigned h01 = pk_bf16(cx[0], cx[1]), h23 = pk_bf16(cx[2], cx[3]);
            if (fr & 2) { h01 = pk_bf16(cx[0] - bflo(h01), cx[1] - bfhi(h01)); h23 = pk_bf16(cx[2] - bflo(h23), cx[3] - bfhi(h23)); }
            if (fr < 4) { v2u d; d.x = h01; d.y = h23; *(LAS v2u*)(cimg + (fr * 160 + 32 * (w >> 1) + 8 * fg + 4 * (w & 1)) * 2) = d; }
        }
        BAR();
        STAMP(10);
        {
            v4u cf[4];
#pragma unroll
            for (int ks = 0; ks < 4; ++ks) cf[ks] = *(LAS v4u*)(cimg + ((fr & 3) * 160 + 32 * ks + 8 * fg) * 2);
#pragma unroll
            for (int ks = 0; ks < 4; ++ks)
#pragma unroll
                for (int ct = 0; ct < 4; ++ct) oacc[ct] = mfma16(wof[ct][ks], cf[ks], oacc[ct]);
#pragma unroll
            for (int ct = 0; ct < 4; ++ct) {
                float o4[4];
#pragma unroll
                for (int e = 0; e < 4; ++e) o4[e] = oacc[ct][e] + dpp<QX2>(oacc[ct][e]);
                const float v0 = (fr & 2) ? o4[2] : o4[0], v1 = (fr & 2) ? o4[3] : o4[1];
                if (fr < 4) put_unit(rs, WS_E6 + (wn * 512 + (fr & 1) * 256 + 32 * w + 8 * ct + 2 * fg + (fr >> 1)) * 16, __float_as_uint(v0), __float_as_uint(v1), ta, tb);
            }
        }
        STAMP(11);
        {
            v4u uf[1];
            unsigned off[1]; off[0] = WS_E6 + ((t >> 2) * 512 + 4 * wn + (t & 3)) * 16;
            poll_units<1>(rs, off, uf, ta, tb);
            float c0 = __uint_as_float(uf[0].x), c1 = __uint_as_float(uf[0].z);
            c0 += dpp<ROR4>(c0); c1 += dpp<ROR4>(c1); c0 += dpp<ROR8>(c0); c1 += dpp<ROR8>(c1);
            c0 = sum_hi(c0); c1 = sum_hi(c1);
            if (lane < 4) { f32x2 d; d[0] = c0; d[1] = c1; *(LAS f32x2*)(fin + w * 8 + 2 * lane) = d; }
            BAR();
            if (t < 4) {
                f32x2 s = {0.f, 0.f};
#pragma unroll
                for (int ww = 0; ww < 8; ++ww) s += *(LAS f32x2*)(fin + ww * 8 + 2 * t);
                *(unsigned*)(out + 8 * wn + 2 * t) = pk_bf16(s[0], s[1]);
                if (bid == 0 && t == 0) { v2u e2; e2.x = ta; e2.y = tb; __builtin_amdgcn_raw_buffer_store_b64(e2, rs, WS_EPOCH, 0, 16); }
            }
        }
        STAMP(12);
    } else {
        const int an = xcd * 16 + (idx - 16), ab = an >> 6, ai = an & 63;
        v4u epv; asm volatile("global_load_dwordx4 %0, %1, off" : "=v"(epv) : "v"(ws + WS_EPOCH) : "memory");
        while (__builtin_amdgcn_s_memrealtime() - t_start < (unsigned long long)CDELAY) __builtin_amdgcn_s_sleep(1);
        {
            const v4u sv = {SENT, SENT, SENT, SENT};
#pragma unroll
            for (int ctp = 0; ctp < 2; ++ctp)
#pragma unroll
                for (int hT = 0; hT < 2; ++hT) {
                    const int hd = 16 * hT + fr;
                    __builtin_amdgcn_raw_buffer_store_b128(sv, rs, (int)(WS_E4 + ((((((hd >> 1) * 4 + (w >> 1)) * 2 + ab) * 64 + ai) * 32) + (w & 1) * 16 + ctp * 8 + (hd & 1) * 4 + fg) * 16), 0, 16);
                }
        }
        while (__builtin_amdgcn_s_memrealtime() - t_start < (unsigned long long)ADELAY) __builtin_amdgcn_s_sleep(1);
        PIN();
        const bf16_t* cbase = ckv + ((size_t)ab * 8192 + ai * 128) * 512;
#define CDMA(r) { const int row = 8 * (r) + w; glds16(cbase + (size_t)row * 512 + ((unsigned)lane ^ sig(row & 15)) * 8, lds_base + L_C + row * 1024); }
#pragma unroll
        for (int r = 0; r < NCA; ++r) CDMA(r);
        v4u uc0 = {0u, 0u, 0u, 0u}, uc1 = {0u, 0u, 0u, 0u};
        if (w == 0) {
            const unsigned char* cp = ws + WS_CLR + lane * 16;
            while (__builtin_amdgcn_s_memrealtime() - t_start < (unsigned long long)TCLR) __builtin_amdgcn_s_sleep(1);
            asm volatile("global_load_dwordx4 %0, %2, off sc1\n\tglobal_load_dwordx4 %1, %2, off offset:1024 sc1" : "+v"(uc0), "+v"(uc1) : "v"(cp) : "memory");
        }
#pragma unroll
        for (int r = NCA; r < 16; ++r) CDMA(r);
        asm volatile("s_waitcnt vmcnt(%3)" : "+v"(epv), "+v"(uc0), "+v"(uc1) : "i"(16 - NCA) : "memory");
        const unsigned ta = epv.x + 1u, tb = epv.y + 1u;
        if (w == 0) {
            const bool ok = (uc0.y == ta) & (uc0.w == tb) & (uc1.y == ta) & (uc1.w == tb);
            if (!__all(ok)) { unsigned off[2]; v4u uc[2]; off[0] = WS_CLR + lane * 16; off[1] = off[0] + 1024; poll_units<2>(rs, off, uc, ta, tb); }
            STAMP(8);
        }
        PIN();
        STAMP(2);
        const int kt = w >> 1, ht = w & 1;
        LAS unsigned char* Pm = L + L_Q;
        LAS float* red = (LAS float*)(L + L_Q + 8704);
        {
            BAR();
            STAMP(9);
            v4u u[4];
            const unsigned o0 = WS_E2 + (ab * 2048 + t) * 16;
#pragma unroll
            for (int r = 0; r < 4; ++r) u[r] = ld_sc1(rs, o0 + r * 8192);
#pragma unroll 1
            for (int spin = 0; spin < SPIN_CAP; ++spin) {
                bool ok = true;
#pragma unroll
                for (int r = 0; r < 4; ++r) ok &= (u[r].x != SENT) & (u[r].y != SENT) & (u[r].z != SENT) & (u[r].w != SENT);
                if (__all(ok)) break;
                __builtin_amdgcn_s_sleep(ASLEEP);
                asm volatile("" ::: "memory");
                if (!ok) {
#pragma unroll
                    for (int r = 0; r < 4; ++r) u[r] = ld_sc1(rs, o0 + r * 8192);
                }
            }
            STAMP(3);
#pragma unroll
            for (int r = 0; r < 4; ++r) {
                const int hd = 8 * r + w;
                *(LAS v4u*)(L + L_Q + hd * 1024 + ((unsigned)lane ^ sig(hd & 15)) * 16) = u[r];
            }
        }
        BAR();
        STAMP(4);
        f32x4 s0 = {0.f, 0.f, 0.f, 0.f}, s1 = {0.f, 0.f, 0.f, 0.f};
        {
            const unsigned sg = sig(fr);
#pragma unroll
            for (int ks = 0; ks < 16; ++ks) {
                const unsigned ch = (unsigned)(4 * ks + fg) ^ sg;
                const v4u bv = *(LAS v4u*)(L + L_Q + (16 * ht + fr) * 1024 + ch * 16);
                const v4u a0 = *(LAS v4u*)(L + L_C + (16 * kt + fr) * 1024 + ch * 16);
                const v4u a1 = *(LAS v4u*)(L + L_C + (16 * kt + 64 + fr) * 1024 + ch * 16);
                s0 = mfma16(a0, bv, s0); s1 = mfma16(a1, bv, s1);
            }
        }
        float mx = fmaxf(fmaxf(fmaxf(s0[0], s0[1]), fmaxf(s0[2], s0[3])), fmaxf(fmaxf(s1[0], s1[1]), fmaxf(s1[2], s1[3])));
        mx = max_hi(mx);
        BAR();
        STAMP(5);
        if (lane < 16) red[kt * 32 + 16 * ht + lane] = mx;
        BAR();
        const float M = fmaxf(fmaxf(red[16 * ht + fr], red[32 + 16 * ht + fr]), fmaxf(red[64 + 16 * ht + fr], red[96 + 16 * ht + fr]));
        {
            v2u d0, d1;
            d0.x = pk_bf16(exp2f(s0[0] - M), exp2f(s0[1] - M)); d0.y = pk_bf16(exp2f(s0[2] - M), exp2f(s0[3] - M));
            d1.x = pk_bf16(exp2f(s1[0] - M), exp2f(s1[1] - M)); d1.y = pk_bf16(exp2f(s1[2] - M), exp2f(s1[3] - M));
            float ls = ((bflo(d0.x) + bfhi(d0.x)) + (bflo(d0.y) + bfhi(d0.y))) + ((bflo(d1.x) + bfhi(d1.x)) + (bflo(d1.y) + bfhi(d1.y)));
            ls = sum_hi(ls);
            if (lane < 16) red[128 + kt * 32 + 16 * ht + lane] = ls;
            *(LAS v2u*)(Pm + (16 * ht + fr) * P_STR + (32 * (kt >> 1) + 8 * fg + 4 * (kt & 1)) * 2) = d0;
            *(LAS v2u*)(Pm + (16 * ht + fr) * P_STR + (32 * ((kt + 4) >> 1) + 8 * fg + 4 * (kt & 1)) * 2) = d1;
        }
        v4u cfr[4][4];
#pragma unroll
        for (int ct = 0; ct < 4; ++ct) {
            const unsigned ch = (unsigned)(8 * w + 2 * ct + (tp >> 1)) ^ sgt;
            LAS unsigned char* base = L + L_C + (4 * fg + tq) * 1024 + ch * 16 + (tp & 1) * 8;
#pragma unroll
            for (int ks = 0; ks < 4; ++ks) cfr[ct][ks] = tr_frag(base + 32 * ks * 1024, base + (32 * ks + 16) * 1024);
        }
        BAR();
        STAMP(6);
        if (t < 32) {
            const float Mh = fmaxf(fmaxf(red[t], red[32 + t]), fmaxf(red[64 + t], red[96 + t]));
            const float Ls = (red[128 + t] + red[160 + t]) + (red[192 + t] + red[224 + t]);
            put_unit(rs, WS_E4S + ((t * 2 + ab) * 64 + ai) * 16, __float_as_uint(Mh), __float_as_uint(Ls), ta, tb);
        }
        {
            v4u pf[2][4];
#pragma unroll
            for (int hT = 0; hT < 2; ++hT)
#pragma unroll
                for (int ks = 0; ks < 4; ++ks) pf[hT][ks] = *(LAS v4u*)(Pm + (16 * hT + fr) * P_STR + (32 * ks + 8 * fg) * 2);
#pragma unroll
            for (int ctp = 0; ctp < 2; ++ctp) {
                f32x4 acc[2][2];
#pragma unroll
                for (int c2 = 0; c2 < 2; ++c2) { acc[c2][0] = (f32x4){0.f, 0.f, 0.f, 0.f}; acc[c2][1] = (f32x4){0.f, 0.f, 0.f, 0.f}; }
#pragma unroll
                for (int ks = 0; ks < 4; ++ks)
#pragma unroll
                    for (int c2 = 0; c2 < 2; ++c2) {
                        acc[c2][0] = mfma16(cfr[2 * ctp + c2][ks], pf[0][ks], acc[c2][0]); acc[c2][1] = mfma16(cfr[2 * ctp + c2][ks], pf[1][ks], acc[c2][1]);
                    }
#pragma unroll
                for (int hT = 0; hT < 2; ++hT) {
                    const int hd = 16 * hT + fr;
                    v4u uo; uo.x = pk_ns(acc[0][hT][0], acc[0][hT][1]); uo.y = pk_ns(acc[0][hT][2], acc[0][hT][3]);
                    uo.z = pk_ns(acc[1][hT][0], acc[1][hT][1]); uo.w = pk_ns(acc[1][hT][2], acc[1][hT][3]);
                    __builtin_amdgcn_raw_buffer_store_b128(uo, rs, (int)(WS_E4 + ((((((hd >> 1) * 4 + (w >> 1)) * 2 + ab) * 64 + ai) * 32) + (w & 1) * 16 + ctp * 8 + (hd & 1) * 4 + fg) * 16), 0, 16);
                }
            }
        }
        STAMP(7);
    }
}

extern "C" void kernel_launch(void* const* d_in, const int* in_sizes, int n_in, void* d_out, int out_size, void* d_ws, size_t ws_size, hipStream_t stream) {
    static bool attr_set = false;
    if (!attr_set) { (void)hipFuncSetAttribute((const void*)mla_kernel, hipFuncAttributeMaxDynamicSharedMemorySize, L_TOTAL); attr_set = true; }
    mla_kernel<<<NWG, NT, L_TOTAL, stream>>>((const bf16_t*)d_in[0], (const bf16_t*)d_in[1], (const bf16_t*)d_in[2], (const bf16_t*)d_in[3],
        (const bf16_t*)d_in[4], (const bf16_t*)d_in[5], (const bf16_t*)d_in[7], (const bf16_t*)d_in[8], (const bf16_t*)d_in[9], (const bf16_t*)d_in[10],
        (bf16_t*)d_out, (unsigned char*)d_ws);
}
```

```cpp
#include <hip/hip_runtime.h>
#include <stdint.h>

#define GAS __attribute__((address_space(1)))
#define LAS __attribute__((address_space(3)))
typedef unsigned short bf16_t;
typedef unsigned v4u __attribute__((ext_vector_type(4)));
typedef unsigned v2u __attribute__((ext_vector_type(2)));
typedef float f32x4 __attribute__((ext_vector_type(4)));
typedef float f32x2 __attribute__((ext_vector_type(2)));
typedef short bf16x8 __attribute__((ext_vector_type(8)));
typedef short v4s __attribute__((ext_vector_type(4)));
typedef __bf16 bf2v __attribute__((ext_vector_type(2)));

#define PSLEEP 2
#define ASLEEP 1
#define TCLR 200
#define QPF 4
#define CLRW 7
#define H6WAIT 20
#define NCA 8
#define ADELAY 110
#define WDELAY 440

constexpr int NWG = 256, NT = 512;
constexpr unsigned WS_EPOCH = 0;
constexpr unsigned WS_E1 = 4096;
constexpr unsigned WS_E2 = WS_E1 + 262144;
constexpr unsigned WS_CLR = WS_E2 + 65536;
constexpr unsigned WS_E4S = WS_E2 + 131072;
constexpr unsigned WS_E7 = WS_E4S + 65536;
constexpr unsigned WS_E6 = WS_E7 + 262144;
constexpr unsigned WS_E4 = WS_E6 + 2097152;
constexpr unsigned WS_END = WS_E4 + 4194304;
constexpr unsigned SENT = 0xFFFFFFFFu;

constexpr int L_C = 0, L_Q = 131072, L_TOTAL = 163840, P_STR = 272;
constexpr int SPIN_CAP = 20000;

__device__ __forceinline__ float bflo(unsigned u) { return __uint_as_float(u << 16); }
__device__ __forceinline__ float bfhi(unsigned u) { return __uint_as_float(u & 0xffff0000u); }
__device__ __forceinline__ float bf2f(bf16_t v) { return __uint_as_float(((unsigned)v) << 16); }
__device__ __forceinline__ unsigned pk_bf16(float lo, float hi) { bf2v r; r[0] = (__bf16)lo; r[1] = (__bf16)hi; return __builtin_bit_cast(unsigned, r); }
__device__ __forceinline__ float rbf(float v) { return bflo(pk_bf16(v, 0.f)); }
__device__ __forceinline__ unsigned pk_ns(float lo, float hi) { const unsigned r = pk_bf16(lo, hi); return r == SENT ? 0x7FC07FC0u : r; }
__device__ __forceinline__ float dot2(unsigned a, unsigned b, float c) { return __builtin_amdgcn_fdot2_f32_bf16(__builtin_bit_cast(bf2v, a), __builtin_bit_cast(bf2v, b), c, false); }
__device__ __forceinline__ unsigned sig(unsigned r) { return ((r & 3u) << 1) | ((((r >> 2) ^ (r >> 3)) & 1u) << 3) | (r >> 3); }
__device__ __forceinline__ f32x4 mfma16(v4u a, v4u b, f32x4 c) {
    return __builtin_amdgcn_mfma_f32_16x16x32_bf16(__builtin_bit_cast(bf16x8, a), __builtin_bit_cast(bf16x8, b), c, 0, 0, 0);
}
__device__ __forceinline__ void glds16(const void* gsrc, unsigned lds_dst) {
    unsigned keep;
    asm volatile("s_mov_b32 %0, m0\n\ts_mov_b32 m0, %2\n\ts_nop 0\n\tglobal_load_lds_dwordx4 %1, off nt\n\ts_mov_b32 m0, %0"
                 : "=&s"(keep) : "v"(gsrc), "s"(lds_dst) : "memory");
}
__device__ __forceinline__ void glds16_plain(const void* gsrc, unsigned lds_dst) {
    unsigned keep;
    asm volatile("s_mov_b32 %0, m0\n\ts_mov_b32 m0, %2\n\ts_nop 0\n\tglobal_load_lds_dwordx4 %1, off\n\ts_mov_b32 m0, %0"
                 : "=&s"(keep) : "v"(gsrc), "s"(lds_dst) : "memory");
}
#define LDNT(p) __builtin_nontemporal_load((const v4u*)(p))
#define BAR() do { asm volatile("s_waitcnt lgkmcnt(0)" ::: "memory"); __builtin_amdgcn_s_barrier(); asm volatile("" ::: "memory"); } while (0)
#define PIN() do { asm volatile("" ::: "memory"); __builtin_amdgcn_sched_barrier(0); } while (0)
#define LWAIT() asm volatile("s_waitcnt lgkmcnt(0)" ::: "memory")
#define VWAIT0() asm volatile("s_waitcnt vmcnt(0)" ::: "memory")
__device__ __forceinline__ float red32(float a, float b) {
    auto r = __builtin_amdgcn_permlane32_swap(__float_as_uint(a), __float_as_uint(b), false, false);
    return __uint_as_float(r[0]) + __uint_as_float(r[1]);
}
__device__ __forceinline__ float red16(float a, float b) {
    auto r = __builtin_amdgcn_permlane16_swap(__float_as_uint(a), __float_as_uint(b), false, false);
    return __uint_as_float(r[0]) + __uint_as_float(r[1]);
}
template <int CTRL> __device__ __forceinline__ float dpp(float v) { return __uint_as_float(__builtin_amdgcn_update_dpp(0, __float_as_uint(v), CTRL, 0xf, 0xf, true)); }
#define QX1 0xB1
#define QX2 0x4E
#define ROR4 0x124
#define ROR8 0x128
__device__ __forceinline__ float max_hi(float v) {
    auto r = __builtin_amdgcn_permlane16_swap(__float_as_uint(v), __float_as_uint(v), false, false);
    v = fmaxf(__uint_as_float(r[0]), __uint_as_float(r[1]));
    auto s = __builtin_amdgcn_permlane32_swap(__float_as_uint(v), __float_as_uint(v), false, false);
    return fmaxf(__uint_as_float(s[0]), __uint_as_float(s[1]));
}
__device__ __forceinline__ float sum_hi(float v) { v = red16(v, v); return red32(v, v); }
__device__ __forceinline__ float max_all(float v) {
    v = fmaxf(v, dpp<QX1>(v)); v = fmaxf(v, dpp<QX2>(v)); v = fmaxf(v, dpp<ROR4>(v)); v = fmaxf(v, dpp<ROR8>(v)); return max_hi(v);
}
__device__ __forceinline__ float sum_all(float v) {
    v += dpp<QX1>(v); v += dpp<QX2>(v); v += dpp<ROR4>(v); v += dpp<ROR8>(v); return sum_hi(v);
}
__device__ __forceinline__ v4u tr_frag(LAS unsigned char* p0, LAS unsigned char* p1) {
    const v2u l2 = __builtin_bit_cast(v2u, __builtin_amdgcn_ds_read_tr16_b64_v4i16((LAS v4s*)p0));
    const v2u h2 = __builtin_bit_cast(v2u, __builtin_amdgcn_ds_read_tr16_b64_v4i16((LAS v4s*)p1));
    v4u a; a.x = l2.x; a.y = l2.y; a.z = h2.x; a.w = h2.y; return a;
}
__device__ __forceinline__ v4u ld_sc1(__amdgpu_buffer_rsrc_t rs, unsigned off) { asm volatile("" : "+v"(off)); return __builtin_amdgcn_raw_buffer_load_b128(rs, (int)off, 0, 16); }
template <int N, int SLP = PSLEEP>
__device__ __forceinline__ void poll_units(__amdgpu_buffer_rsrc_t rs, const unsigned (&off)[N], v4u (&u)[N], unsigned ta, unsigned tb) {
#pragma unroll
    for (int i = 0; i < N; ++i) u[i] = ld_sc1(rs, off[i]);
#pragma unroll 1
    for (int spin = 0; spin < SPIN_CAP; ++spin) {
        bool ok = true;
#pragma unroll
        for (int i = 0; i < N; ++i) ok &= (u[i].y == ta) & (u[i].w == tb);
        if (__all(ok)) break;
        __builtin_amdgcn_s_sleep(SLP);
        asm volatile("" ::: "memory");
        if (!ok) {
#pragma unroll
            for (int i = 0; i < N; ++i) u[i] = ld_sc1(rs, off[i]);
        }
    }
}
__device__ __forceinline__ void put_unit(__amdgpu_buffer_rsrc_t rs, unsigned off, unsigned v0, unsigned v1, unsigned ta, unsigned tb) {
    v4u u; u.x = v0; u.y = ta; u.z = v1; u.w = tb;
    __builtin_amdgcn_raw_buffer_store_b128(u, rs, (int)off, 0, 16);
}

__global__ void __launch_bounds__(NT, 2)
mla_kernel(const bf16_t* __restrict__ x, const bf16_t* __restrict__ ckv, const bf16_t* __restrict__ wq, const bf16_t* __restrict__ wuk,
           const bf16_t* __restrict__ wuv, const bf16_t* __restrict__ wo, const bf16_t* __restrict__ wg, const bf16_t* __restrict__ wu,
           const bf16_t* __restrict__ wd, const bf16_t* __restrict__ gw, bf16_t* out, unsigned char* ws) {
    extern __shared__ __attribute__((aligned(16))) unsigned char smem[];
    LAS unsigned char* L = (LAS unsigned char*)smem;
    const unsigned lds_base = (unsigned)(size_t)L;
    const int t = threadIdx.x, lane = t & 63, w = __builtin_amdgcn_readfirstlane(t >> 6);
    const int bid = blockIdx.x, xcd = bid & 7, idx = bid >> 3;
    const unsigned long long t_start = __builtin_amdgcn_s_memrealtime();
    asm volatile("" :: "s"(x), "s"(ckv), "s"(wq), "s"(wuk), "s"(wuv), "s"(wo), "s"(wg), "s"(wu), "s"(wd), "s"(gw), "s"(out), "s"(ws));
    const __amdgpu_buffer_rsrc_t rs = __builtin_amdgcn_make_buffer_rsrc((void*)ws, 0, (int)WS_END, 0x00020000);
    const int fr = lane & 15, fg = lane >> 4;
    const int q16 = t & 15, r32 = t >> 4;
    const int tq = fr >> 2, tp = fr & 3;
    const unsigned sgt = sig(4 * fg + tq);

    if (__builtin_expect(idx < 16, 1)) {
        const int h1 = xcd * 4 + (idx >> 2), j1 = idx & 3, wn = xcd * 16 + idx;
        LAS float* r1 = (LAS float*)(L + L_Q);
        LAS bf16_t* oimg = (LAS bf16_t*)(L + L_Q + 8704);
        LAS float* fin = (LAS float*)(L + L_Q + 10240);
        LAS unsigned char* qimg = L + L_Q + 12288;
        const v2u ep = __builtin_amdgcn_raw_buffer_load_b64(rs, WS_EPOCH, 0, 0);
        if (w == 1 && lane < 32) {
            const v4u sv = {SENT, SENT, SENT, SENT};
            __builtin_amdgcn_raw_buffer_store_b128(sv, rs, (int)(WS_E2 + ((lane >> 4) * 32 + h1) * 1024 + 256 * j1 + (lane & 15) * 16), 0, 16);
        }
        PIN();
        const bf16_t* wqp = wq + (size_t)(128 * j1 + 4 * r32) * 4096 + h1 * 128 + 8 * q16;
        const v4u wq0 = LDNT(wqp), wq1 = LDNT(wqp + 4096), wq2 = LDNT(wqp + 8192), wq3 = LDNT(wqp + 12288);
        const v2u xa = *(const v2u*)(x + 128 * j1 + 4 * r32), xb = *(const v2u*)(x + 512 + 128 * j1 + 4 * r32);
        PIN();
        __builtin_amdgcn_s_barrier();
        const unsigned ta = ep.x + 1u, tb = ep.y + 1u;
        if (w == 1) {
            asm volatile("s_waitcnt vmcnt(4)" ::: "memory");
            if (lane == 0) put_unit(rs, WS_CLR + wn * 16, 0u, 0u, ta, tb);
        }
        PIN();
        v4u ukf[4];
#pragma unroll
        for (int ks = 0; ks < 4; ++ks) ukf[ks] = LDNT(wuk + ((size_t)(128 * j1 + 16 * w + fr) * 32 + h1) * 128 + 32 * ks + 8 * fg);
        const bf16_t xg0 = x[t], xg1 = x[512 + t], gwr = gw[t];
        PIN();
        {
            float a[16];
#pragma unroll
            for (int e = 0; e < 4; ++e) {
                const unsigned lo = __builtin_amdgcn_perm(wq1[e], wq0[e], 0x05040100u), hi = __builtin_amdgcn_perm(wq1[e], wq0[e], 0x07060302u);
                const unsigned lo2 = __builtin_amdgcn_perm(wq3[e], wq2[e], 0x05040100u), hi2 = __builtin_amdgcn_perm(wq3[e], wq2[e], 0x07060302u);
                a[2 * e] = dot2(lo2, xa.y, dot2(lo, xa.x, 0.f)); a[2 * e + 1] = dot2(hi2, xa.y, dot2(hi, xa.x, 0.f));
                a[8 + 2 * e] = dot2(lo2, xb.y, dot2(lo, xb.x, 0.f)); a[8 + 2 * e + 1] = dot2(hi2, xb.y, dot2(hi, xb.x, 0.f));
            }
            float r8[8], r4[4];
#pragma unroll
            for (int i = 0; i < 8; ++i) r8[i] = red32(a[i], a[8 + i]);
#pragma unroll
            for (int i = 0; i < 4; ++i) r4[i] = red16(r8[i], r8[4 + i]);
            const int b = lane >> 5, d = 8 * q16 + 4 * ((lane >> 4) & 1);
            f32x4 dd; dd[0] = r4[0]; dd[1] = r4[1]; dd[2] = r4[2]; dd[3] = r4[3];
            *(LAS f32x4*)(r1 + (w * 2 + b) * 128 + d) = dd;
        }
        BAR();
        if (w == 0) {
            f32x4 s = {0.f, 0.f, 0.f, 0.f};
#pragma unroll
            for (int ww = 0; ww < 8; ++ww) s += *(LAS f32x4*)(r1 + ww * 256 + 4 * lane);
            put_unit(rs, WS_E1 + ((h1 * 4 + j1) * 64 + lane) * 16, pk_bf16(s[0], s[1]), pk_bf16(s[2], s[3]), ta, tb);
            unsigned off[4]; v4u u[4];
#pragma unroll
            for (int r = 0; r < 4; ++r) off[r] = WS_E1 + ((h1 * 4 + r) * 64 + lane) * 16;
            poll_units<4>(rs, off, u, ta, tb);
#pragma unroll
            for (int r = 0; r < 4; ++r) { v2u d; d.x = u[r].x; d.y = u[r].z; *(LAS v2u*)(qimg + (2 * r + (lane >> 5)) * 272 + (lane & 31) * 8) = d; }
        }
        const float gg0 = sum_all(rbf(bf2f(xg0) * bf2f(gwr))), gg1 = sum_all(rbf(bf2f(xg1) * bf2f(gwr)));
        BAR();
        {
            f32x4 acc = {0.f, 0.f, 0.f, 0.f};
#pragma unroll
            for (int ks = 0; ks < 4; ++ks) {
                const v4u b = *(LAS v4u*)(qimg + (fr & 7) * 272 + (32 * ks + 8 * fg) * 2);
                acc = mfma16(ukf[ks], b, acc);
            }
            float qa[4];
#pragma unroll
            for (int e = 0; e < 4; ++e) { float v = acc[e]; v += dpp<QX2>(v); v += dpp<ROR4>(v); qa[e] = v; }
            LAS unsigned char* e2i = L + L_Q + 9728;
            if (fr < 2) { v2u d; d.x = pk_ns(qa[0], qa[1]); d.y = pk_ns(qa[2], qa[3]); *(LAS v2u*)(e2i + fr * 256 + (16 * w + 4 * fg) * 2) = d; }
            BAR();
            if (t < 32) {
                const v4u d = *(LAS v4u*)(e2i + t * 16);
                __builtin_amdgcn_raw_buffer_store_b128(d, rs, (int)(WS_E2 + ((t >> 4) * 32 + h1) * 1024 + 256 * j1 + (t & 15) * 16), 0, 16);
            }
        }
        float ga;
        {
            LAS float* gts = (LAS float*)(L + L_Q + 8192);
            if (lane == 0) { gts[2 * w] = gg0; gts[2 * w + 1] = gg1; }
            BAR();
            float s = 0.f;
#pragma unroll
            for (int ww = 0; ww < 8; ++ww) s += gts[2 * ww + (xcd >> 2)];
            ga = rbf(1.f / (1.f + __expf(-rbf(s))));
        }
        while (__builtin_amdgcn_s_memrealtime() - t_start < (unsigned long long)WDELAY) __builtin_amdgcn_s_sleep(2);
        BAR();
#pragma unroll
        for (int r = 0; r < 4; ++r) {
            const int row = 16 * w + 4 * r + (lane >> 4);
            glds16(wuv + ((size_t)(128 * j1 + row) * 32 + h1) * 128 + ((unsigned)(lane & 15) ^ sig(row & 15)) * 8, lds_base + L_Q + (16 * w + 4 * r) * 256);
        }
        const bf16_t* wop = wo + (size_t)(h1 * 128) * 512;
#define WODMA(row) glds16(wop + (size_t)(row) * 512 + ((unsigned)lane ^ sig((row) & 15)) * 8, lds_base + L_C + (row) * 1024)
#pragma unroll
        for (int r = 0; r < 16; ++r) { const int rr = (r + 4 * j1) & 15; WODMA(8 * rr + w); }
        VWAIT0();
        BAR();
        v4u xf[4][4];
        {
            const unsigned sgn0 = sig((8 * fg + tq) & 15), sgn1 = sig((8 * fg + 4 + tq) & 15);
            LAS unsigned char* ub = L + L_Q + fr * 256;
            const unsigned sgf = sig(fr);
            v4u bf[4][4];
#pragma unroll
            for (int mt = 0; mt < 4; ++mt) {
                const unsigned cc = (unsigned)(8 * w + 2 * mt + (tp >> 1));
#pragma unroll
                for (int ks = 0; ks < 4; ++ks)
                    bf[mt][ks] = tr_frag(L + L_C + (32 * ks + 8 * fg + tq) * 1024 + (cc ^ sgn0) * 16 + (tp & 1) * 8, L + L_C + (32 * ks + 8 * fg + 4 + tq) * 1024 + (cc ^ sgn1) * 16 + (tp & 1) * 8);
            }
#pragma unroll
            for (int kp = 0; kp < 4; ++kp) {
                f32x4 xa[2][4];
#pragma unroll
                for (int c2 = 0; c2 < 2; ++c2) {
#pragma unroll
                    for (int mt = 0; mt < 4; ++mt) xa[c2][mt] = (f32x4){0.f, 0.f, 0.f, 0.f};
#pragma unroll
                    for (int ks = 0; ks < 4; ++ks) {
                        const v4u af = *(LAS v4u*)(ub + (2 * kp + c2) * 4096 + (((unsigned)(4 * ks + fg)) ^ sgf) * 16);
#pragma unroll
                        for (int mt = 0; mt < 4; ++mt) xa[c2][mt] = mfma16(af, bf[mt][ks], xa[c2][mt]);
                    }
                }
#pragma unroll
                for (int mt = 0; mt < 4; ++mt) {
                    v4u f; f.x = pk_bf16(xa[0][mt][0], xa[0][mt][1]); f.y = pk_bf16(xa[0][mt][2], xa[0][mt][3]);
                    f.z = pk_bf16(xa[1][mt][0], xa[1][mt][1]); f.w = pk_bf16(xa[1][mt][2], xa[1][mt][3]);
                    xf[mt][kp] = f;
                }
                PIN();
            }
        }
        {
            v4u u[4];
            const unsigned o0 = WS_E4 + ((((h1 >> 1) * 4 + j1) * 128 + r32) * 32 + (q16 >> 2) * 8 + (h1 & 1) * 4 + (q16 & 3)) * 16;
            LAS float* scl = (LAS float*)(L + L_C);
            BAR();
            if (w == 0) {
                const unsigned s0o = WS_E4S + ((h1 * 2) * 64 + lane) * 16;
                unsigned off[2] = {s0o, s0o + 1024}; v4u st[2];
                poll_units<2>(rs, off, st, ta, tb);
#pragma unroll
                for (int b = 0; b < 2; ++b) {
                    const float mi = __uint_as_float(st[b].x), li = __uint_as_float(st[b].z);
                    const float wi = exp2f(mi - max_all(mi));
                    scl[b * 64 + lane] = wi / sum_all(wi * li);
                }
            }
            BAR();
#pragma unroll
            for (int r = 0; r < 4; ++r) u[r] = ld_sc1(rs, o0 + r * 32 * 512);
            const float s00 = scl[r32], s01 = scl[r32 + 32], s10 = scl[64 + r32], s11 = scl[96 + r32];
#pragma unroll 1
            for (int spin = 0; spin < SPIN_CAP; ++spin) {
                bool ok = true;
#pragma unroll
                for (int r = 0; r < 4; ++r) ok &= (u[r].x != SENT) & (u[r].y != SENT) & (u[r].z != SENT) & (u[r].w != SENT);
                if (__all(ok)) break;
                __builtin_amdgcn_s_sleep(1);
                asm volatile("" ::: "memory");
                if (!ok) {
#pragma unroll
                    for (int r = 0; r < 4; ++r) u[r] = ld_sc1(rs, o0 + r * 32 * 512);
                }
            }
            float o[16];
#pragma unroll
            for (int e = 0; e < 4; ++e) {
                o[2 * e] = s00 * bflo(u[0][e]) + s01 * bflo(u[1][e]); o[2 * e + 1] = s00 * bfhi(u[0][e]) + s01 * bfhi(u[1][e]);
                o[8 + 2 * e] = s10 * bflo(u[2][e]) + s11 * bflo(u[3][e]); o[8 + 2 * e + 1] = s10 * bfhi(u[2][e]) + s11 * bfhi(u[3][e]);
            }
            float r8[8], r4[4];
#pragma unroll
            for (int i = 0; i < 8; ++i) r8[i] = red32(o[i], o[8 + i]);
#pragma unroll
            for (int i = 0; i < 4; ++i) r4[i] = red16(r8[i], r8[4 + i]);
            const int c = 64 * (q16 >> 3) + 16 * (2 * ((q16 >> 2) & 1) + ((lane >> 4) & 1)) + 4 * (q16 & 3);
            f32x4 dd; dd[0] = r4[0]; dd[1] = r4[1]; dd[2] = r4[2]; dd[3] = r4[3];
            *(LAS f32x4*)(r1 + (w * 2 + (lane >> 5)) * 128 + c) = dd;
        }
        BAR();
        {
            f32x4 s = {0.f, 0.f, 0.f, 0.f};
#pragma unroll
            for (int ww = 0; ww < 8; ++ww) s += *(LAS f32x4*)(r1 + ww * 256 + 4 * lane);
            v2u hp, lp;
            hp.x = pk_bf16(s[0], s[1]); hp.y = pk_bf16(s[2], s[3]);
            lp.x = pk_bf16(s[0] - bflo(hp.x), s[1] - bfhi(hp.x)); lp.y = pk_bf16(s[2] - bflo(hp.y), s[3] - bfhi(hp.y));
            const int c = 4 * (lane & 31), bb = lane >> 5;
            const int pos = 32 * (c >> 5) + 8 * ((c >> 2) & 3) + 4 * ((c >> 4) & 1);
            *(LAS v2u*)(oimg + bb * 128 + pos) = hp;
            *(LAS v2u*)(oimg + (2 + bb) * 128 + pos) = lp;
            LWAIT();
            f32x4 oacc[4];
#pragma unroll
            for (int mt = 0; mt < 4; ++mt) oacc[mt] = (f32x4){0.f, 0.f, 0.f, 0.f};
#pragma unroll
            for (int kp = 0; kp < 4; ++kp) {
                const v4u af = *(LAS v4u*)(oimg + (fr & 3) * 128 + 32 * kp + 8 * fg);
#pragma unroll
                for (int mt = 0; mt < 4; ++mt) oacc[mt] = mfma16(af, xf[mt][kp], oacc[mt]);
            }
            const f32x4 o = fg == 0 ? oacc[0] : fg == 1 ? oacc[1] : fg == 2 ? oacc[2] : oacc[3];
            const float o0 = o[0] + o[2], o1 = o[1] + o[3];
            const float n0 = dpp<QX1>(o0), n1 = dpp<QX1>(o1);
            const float v0 = (fr & 1) ? n1 : o0, v1 = (fr & 1) ? o1 : n0;
            put_unit(rs, WS_E6 + (wn * 512 + (fr & 1) * 256 + 32 * w + 8 * fg + (fr >> 1)) * 16, __float_as_uint(v0), __float_as_uint(v1), ta, tb);
        }
        if ((idx & 1) == 0) {
            const int k = xcd * 8 + (idx >> 1);
            v4u uf[4]; unsigned off[4];
#pragma unroll
            for (int r = 0; r < 4; ++r) off[r] = WS_E6 + (((t >> 3) + 64 * r) * 512 + 8 * k + (t & 7)) * 16;
            __builtin_amdgcn_s_sleep(H6WAIT);
            poll_units<4, 1>(rs, off, uf, ta, tb);
            f32x4 c;
            c[0] = __uint_as_float(uf[0].x) + __uint_as_float(uf[1].x); c[1] = __uint_as_float(uf[0].z) + __uint_as_float(uf[1].z);
            c[2] = __uint_as_float(uf[2].x) + __uint_as_float(uf[3].x); c[3] = __uint_as_float(uf[2].z) + __uint_as_float(uf[3].z);
#pragma unroll
            for (int e = 0; e < 4; ++e) { float v = c[e]; v += dpp<ROR8>(v); c[e] = sum_hi(v); }
            if (lane < 8) *(LAS f32x4*)(fin + w * 32 + 4 * lane) = c;
            BAR();
            if (t < 8) {
                f32x4 sf = {0.f, 0.f, 0.f, 0.f};
#pragma unroll
                for (int ww = 0; ww < 8; ++ww) sf += *(LAS f32x4*)(fin + ww * 32 + 4 * t);
                const float og = rbf(1.f - ga);
                *(unsigned*)(out + 16 * k + 2 * t) = pk_bf16(rbf(ga * rbf(sf[0])) + rbf(og * rbf(sf[2])), rbf(ga * rbf(sf[1])) + rbf(og * rbf(sf[3])));
                if (bid == 0 && t == 0) { v2u e2; e2.x = ta; e2.y = tb; __builtin_amdgcn_raw_buffer_store_b64(e2, rs, WS_EPOCH, 0, 16); }
            }
        }
    } else {
        const int an = xcd * 16 + (idx - 16), ab = an >> 6, ai = an & 63;
        v4u epv; asm volatile("global_load_dwordx4 %0, %1, off" : "=v"(epv) : "v"(ws + WS_EPOCH) : "memory");
        while (__builtin_amdgcn_s_memrealtime() - t_start < (unsigned long long)ADELAY) __builtin_amdgcn_s_sleep(1);
        PIN();
        const bf16_t* cbase = ckv + ((size_t)ab * 8192 + ai * 128) * 512;
#define CDMA(r) { const int row = 8 * (r) + w; glds16(cbase + (size_t)row * 512 + ((unsigned)lane ^ sig(row & 15)) * 8, lds_base + L_C + row * 1024); }
#pragma unroll
        for (int r = 0; r < NCA; ++r) CDMA(r);
        v4u uc0 = {0u, 0u, 0u, 0u}, uc1 = {0u, 0u, 0u, 0u};
        if (w == CLRW) {
            const unsigned char* cp = ws + WS_CLR + lane * 16;
            while (__builtin_amdgcn_s_memrealtime() - t_start < (unsigned long long)TCLR) __builtin_amdgcn_s_sleep(1);
            asm volatile("global_load_dwordx4 %0, %2, off sc1\n\tglobal_load_dwordx4 %1, %2, off offset:1024 sc1" : "+v"(uc0), "+v"(uc1) : "v"(cp) : "memory");
        }
#pragma unroll
        for (int r = NCA; r < 16; ++r) CDMA(r);
        asm volatile("s_waitcnt vmcnt(%3)" : "+v"(epv), "+v"(uc0), "+v"(uc1) : "i"(16 - NCA) : "memory");
        const unsigned ta = epv.x + 1u, tb = epv.y + 1u;
        if (w == CLRW) {
            const bool ok = (uc0.y == ta) & (uc0.w == tb) & (uc1.y == ta) & (uc1.w == tb);
            if (!__all(ok)) { unsigned off[2]; v4u uc[2]; off[0] = WS_CLR + lane * 16; off[1] = off[0] + 1024; poll_units<2>(rs, off, uc, ta, tb); asm volatile("" :: "v"(uc[0]), "v"(uc[1])); }
        }
        PIN();
        const int kt = w >> 1, ht = w & 1;
        LAS unsigned char* Pm = L + L_C;
        LAS float* red = (LAS float*)(L + L_C + 8704);
        constexpr int L_G = L_C + 16384;
        constexpr int L_D = L_C + 49152;
        constexpr int L_X = L_C + 12288;
        {
            const v4u sv = {SENT, SENT, SENT, SENT};
#pragma unroll
            for (int ctp = 0; ctp < 2; ++ctp)
#pragma unroll
                for (int hT = 0; hT < 2; ++hT) {
                    const int hd = 16 * hT + fr;
                    __builtin_amdgcn_raw_buffer_store_b128(sv, rs, (int)(WS_E4 + ((((((hd >> 1) * 4 + (w >> 1)) * 2 + ab) * 64 + ai) * 32) + (w & 1) * 16 + ctp * 8 + (hd & 1) * 4 + fg) * 16), 0, 16);
                }
        }
        PIN();
        BAR();
        v4u u[4];
        const unsigned o0 = WS_E2 + (ab * 2048 + t) * 16;
        const unsigned char* qp = ws + o0;
        {
            asm volatile("global_load_dwordx4 %0, %4, off sc1\n\tglobal_load_dwordx4 %1, %5, off sc1\n\tglobal_load_dwordx4 %2, %6, off sc1\n\tglobal_load_dwordx4 %3, %7, off sc1\n\ts_waitcnt vmcnt(8)"
                         : "=&v"(u[0]), "=&v"(u[1]), "=&v"(u[2]), "=&v"(u[3]) : "v"(qp), "v"(qp + 8192), "v"(qp + 16384), "v"(qp + 24576) : "memory");
        }
        BAR();
#define QWAIT() asm volatile("s_waitcnt vmcnt(0)" : "+v"(u[0]), "+v"(u[1]), "+v"(u[2]), "+v"(u[3]) :: "memory")
#define QRETRY() { QWAIT(); bool ok = true; _Pragma("unroll") for (int r = 0; r < 4; ++r) ok &= (u[r].x != SENT) & (u[r].y != SENT) & (u[r].z != SENT) & (u[r].w != SENT); \
                   if (!ok) asm volatile("global_load_dwordx4 %0, %4, off sc1\n\tglobal_load_dwordx4 %1, %5, off sc1\n\tglobal_load_dwordx4 %2, %6, off sc1\n\tglobal_load_dwordx4 %3, %7, off sc1" \
                         : "+v"(u[0]), "+v"(u[1]), "+v"(u[2]), "+v"(u[3]) : "v"(qp), "v"(qp + 8192), "v"(qp + 16384), "v"(qp + 24576) : "memory"); PIN(); }
        v4u af0[16], af1[16], cfr[4][4];
        unsigned fo[4];
#pragma unroll
        for (int k4 = 0; k4 < 4; ++k4) { fo[k4] = fr * 1024 + (((unsigned)(4 * k4 + fg)) ^ sig(fr)) * 16; asm volatile("" : "+v"(fo[k4])); }
        {
#pragma unroll
            for (int ks = 0; ks < 16; ++ks) {
                LAS unsigned char* pa = L + L_C + (16 * kt) * 1024 + fo[ks & 3] + (ks >> 2) * 256;
                af0[ks] = *(LAS v4u*)(pa); af1[ks] = *(LAS v4u*)(pa + 65536);
            }
            LWAIT();
            QRETRY();
#pragma unroll
            for (int ct = 0; ct < 4; ++ct) {
                const unsigned ch = (unsigned)(8 * w + 2 * ct + (tp >> 1)) ^ sgt;
                LAS unsigned char* base = L + L_C + (4 * fg + tq) * 1024 + ch * 16 + (tp & 1) * 8;
#pragma unroll
                for (int ks = 0; ks < 4; ++ks) cfr[ct][ks] = tr_frag(base + 32 * ks * 1024, base + (32 * ks + 16) * 1024);
            }
            LWAIT();
        }
        const bool has = an < 112;
        const int kq = an & 3, cb = has ? (an >> 2) : 0, f0 = 64 * cb + 16 * kq;
        const bf16_t* gsrc = ((lane & 8) ? wu : wg) + (size_t)(128 * kq + 32 * (w & 3) + (lane >> 4)) * 1792 + 64 * cb + 8 * (lane & 7);
        const bf16_t* dsrc = wd + (size_t)(f0 + 4 * (w & 3)) * 512 + 8 * lane;
        asm volatile("" : "+v"(gsrc), "+v"(dsrc));
        QWAIT();
#pragma unroll 1
        for (int spin = 0; spin < SPIN_CAP; ++spin) {
            bool ok = true;
#pragma unroll
            for (int r = 0; r < 4; ++r) ok &= (u[r].x != SENT) & (u[r].y != SENT) & (u[r].z != SENT) & (u[r].w != SENT);
            if (__all(ok)) break;
            __builtin_amdgcn_s_sleep(ASLEEP);
            asm volatile("" ::: "memory");
            if (!ok) {
#pragma unroll
                for (int r = 0; r < 4; ++r) u[r] = ld_sc1(rs, o0 + r * 8192);
            }
        }
#pragma unroll
        for (int r = 0; r < 4; ++r) {
            const int hd = 8 * r + w;
            *(LAS v4u*)(L + L_Q + hd * 1024 + ((unsigned)lane ^ sig(hd & 15)) * 16) = u[r];
        }
        BAR();
        PIN();
        f32x4 s0 = {0.f, 0.f, 0.f, 0.f}, s1 = {0.f, 0.f, 0.f, 0.f};
        {
            LAS unsigned char* qb = L + L_Q + (16 * ht) * 1024;
            v4u bq[QPF];
#pragma unroll
            for (int i = 0; i < QPF; ++i) bq[i] = *(LAS v4u*)(qb + fo[i & 3] + (i >> 2) * 256);
            __builtin_amdgcn_sched_barrier(0);
#pragma unroll
            for (int ks = 0; ks < 16; ++ks) {
                const v4u bv = bq[ks % QPF];
                s0 = mfma16(af0[ks], bv, s0); s1 = mfma16(af1[ks], bv, s1);
                if (ks + QPF < 16) bq[ks % QPF] = *(LAS v4u*)(qb + fo[(ks + QPF) & 3] + ((ks + QPF) >> 2) * 256);
                __builtin_amdgcn_sched_barrier(0);
            }
        }
#pragma unroll
        for (int e = 0; e < 4; e += 2) {
            const unsigned a0 = pk_bf16(s0[e], s0[e + 1]), a1 = pk_bf16(s1[e], s1[e + 1]);
            const unsigned b0 = pk_bf16(bflo(a0) * 0.08838834764831845f, bfhi(a0) * 0.08838834764831845f), b1 = pk_bf16(bflo(a1) * 0.08838834764831845f, bfhi(a1) * 0.08838834764831845f);
            s0[e] = bflo(b0) * 1.4426950408889634f; s0[e + 1] = bfhi(b0) * 1.4426950408889634f; s1[e] = bflo(b1) * 1.4426950408889634f; s1[e + 1] = bfhi(b1) * 1.4426950408889634f;
        }
        float mx = fmaxf(fmaxf(fmaxf(s0[0], s0[1]), fmaxf(s0[2], s0[3])), fmaxf(fmaxf(s1[0], s1[1]), fmaxf(s1[2], s1[3])));
        mx = max_hi(mx);
        if (lane < 16) red[kt * 32 + 16 * ht + lane] = mx;
        if (w < 4) {
#pragma unroll
            for (int i = 0; i < 8; ++i) glds16(gsrc + (size_t)(4 * i) * 1792, lds_base + L_G + (32 * w + 4 * i) * 256);
#pragma unroll
            for (int i = 0; i < 4; ++i) glds16(dsrc + 512 * i, lds_base + L_D + (4 * w + i) * 1024);
            if (w < 2) glds16_plain(x + 512 * w + 8 * lane, lds_base + L_X + w * 1024);
        }
        BAR();
        const float M = fmaxf(fmaxf(red[16 * ht + fr], red[32 + 16 * ht + fr]), fmaxf(red[64 + 16 * ht + fr], red[96 + 16 * ht + fr]));
        {
            v2u d0, d1;
            d0.x = pk_bf16(exp2f(s0[0] - M), exp2f(s0[1] - M)); d0.y = pk_bf16(exp2f(s0[2] - M), exp2f(s0[3] - M));
            d1.x = pk_bf16(exp2f(s1[0] - M), exp2f(s1[1] - M)); d1.y = pk_bf16(exp2f(s1[2] - M), exp2f(s1[3] - M));
            float ls = ((bflo(d0.x) + bfhi(d0.x)) + (bflo(d0.y) + bfhi(d0.y))) + ((bflo(d1.x) + bfhi(d1.x)) + (bflo(d1.y) + bfhi(d1.y)));
            ls = sum_hi(ls);
            if (lane < 16) red[128 + kt * 32 + 16 * ht + lane] = ls;
            *(LAS v2u*)(Pm + (16 * ht + fr) * P_STR + (32 * (kt >> 1) + 8 * fg + 4 * (kt & 1)) * 2) = d0;
            *(LAS v2u*)(Pm + (16 * ht + fr) * P_STR + (32 * ((kt + 4) >> 1) + 8 * fg + 4 * (kt & 1)) * 2) = d1;
        }
        BAR();
        if (t < 32) {
            const float Mh = fmaxf(fmaxf(red[t], red[32 + t]), fmaxf(red[64 + t], red[96 + t]));
            const float Ls = (red[128 + t] + red[160 + t]) + (red[192 + t] + red[224 + t]);
            put_unit(rs, WS_E4S + ((t * 2 + ab) * 64 + ai) * 16, __float_as_uint(Mh), __float_as_uint(Ls), ta, tb);
        }
        {
            v4u pf[2][4];
#pragma unroll
            for (int hT = 0; hT < 2; ++hT)
#pragma unroll
                for (int ks = 0; ks < 4; ++ks) pf[hT][ks] = *(LAS v4u*)(Pm + (16 * hT + fr) * P_STR + (32 * ks + 8 * fg) * 2);
#pragma unroll
            for (int ctp = 0; ctp < 2; ++ctp) {
                f32x4 acc[2][2];
#pragma unroll
                for (int c2 = 0; c2 < 2; ++c2) { acc[c2][0] = (f32x4){0.f, 0.f, 0.f, 0.f}; acc[c2][1] = (f32x4){0.f, 0.f, 0.f, 0.f}; }
#pragma unroll
                for (int ks = 0; ks < 4; ++ks)
#pragma unroll
                    for (int c2 = 0; c2 < 2; ++c2) {
                        acc[c2][0] = mfma16(cfr[2 * ctp + c2][ks], pf[0][ks], acc[c2][0]); acc[c2][1] = mfma16(cfr[2 * ctp + c2][ks], pf[1][ks], acc[c2][1]);
                    }
#pragma unroll
                for (int hT = 0; hT < 2; ++hT) {
                    const int hd = 16 * hT + fr;
                    v4u uo; uo.x = pk_ns(acc[0][hT][0], acc[0][hT][1]); uo.y = pk_ns(acc[0][hT][2], acc[0][hT][3]);
                    uo.z = pk_ns(acc[1][hT][0], acc[1][hT][1]); uo.w = pk_ns(acc[1][hT][2], acc[1][hT][3]);
                    __builtin_amdgcn_raw_buffer_store_b128(uo, rs, (int)(WS_E4 + ((((((hd >> 1) * 4 + (w >> 1)) * 2 + ab) * 64 + ai) * 32) + (w & 1) * 16 + ctp * 8 + (hd & 1) * 4 + fg) * 16), 0, 16);
                }
            }
        }
        PIN();
        asm volatile("s_waitcnt vmcnt(4)" ::: "memory");
        BAR();
        const v2u xa = *(LAS v2u*)(L + L_X + (128 * kq + 4 * r32) * 2), xb = *(LAS v2u*)(L + L_X + 1024 + (128 * kq + 4 * r32) * 2);
        LAS unsigned char* gl = L + L_G + (4 * r32) * 256 + q16 * 16;
        const v4u g0 = *(LAS v4u*)(gl), g1 = *(LAS v4u*)(gl + 256), g2 = *(LAS v4u*)(gl + 512), g3 = *(LAS v4u*)(gl + 768);
        const v4u d0 = *(LAS v4u*)(L + L_D + (2 * w) * 1024 + lane * 16), d1 = *(LAS v4u*)(L + L_D + (2 * w + 1) * 1024 + lane * 16);
        {
            float a[16];
#pragma unroll
            for (int e = 0; e < 4; ++e) {
                const unsigned lo = __builtin_amdgcn_perm(g1[e], g0[e], 0x05040100u), hi = __builtin_amdgcn_perm(g1[e], g0[e], 0x07060302u);
                const unsigned lo2 = __builtin_amdgcn_perm(g3[e], g2[e], 0x05040100u), hi2 = __builtin_amdgcn_perm(g3[e], g2[e], 0x07060302u);
                a[2 * e] = dot2(lo2, xa.y, dot2(lo, xa.x, 0.f)); a[2 * e + 1] = dot2(hi2, xa.y, dot2(hi, xa.x, 0.f));
                a[8 + 2 * e] = dot2(lo2, xb.y, dot2(lo, xb.x, 0.f)); a[8 + 2 * e + 1] = dot2(hi2, xb.y, dot2(hi, xb.x, 0.f));
            }
            float r8[8], r4[4];
#pragma unroll
            for (int i = 0; i < 8; ++i) r8[i] = red32(a[i], a[8 + i]);
#pragma unroll
            for (int i = 0; i < 4; ++i) r4[i] = red16(r8[i], r8[4 + i]);
            LAS float* r1 = (LAS float*)(L + L_Q);
            LAS float* hs = (LAS float*)(L + L_Q + 8448);
            const int b = lane >> 5, d = 8 * q16 + 4 * ((lane >> 4) & 1);
            f32x4 dd; dd[0] = r4[0]; dd[1] = r4[1]; dd[2] = r4[2]; dd[3] = r4[3];
            *(LAS f32x4*)(r1 + (w * 2 + b) * 128 + d) = dd;
            BAR();
            if (w < 2) {
                f32x2 sm = {0.f, 0.f};
#pragma unroll
                for (int ww = 0; ww < 8; ++ww) sm += *(LAS f32x2*)(r1 + ww * 256 + 2 * t);
                put_unit(rs, WS_E7 + (an * 128 + t) * 16, __float_as_uint(sm[0]), __float_as_uint(sm[1]), ta, tb);
            }
            if (w == 0) {
                const int kq2 = lane >> 5, bb = (lane >> 4) & 1, mat = (lane >> 3) & 1, cp = lane & 7;
                unsigned off[2]; v4u u[2];
                off[0] = WS_E7 + (((an >> 2) * 4 + kq2) * 128 + bb * 64 + mat * 32 + 8 * kq + cp) * 16; off[1] = off[0] + 2 * 128 * 16;
                poll_units<2>(rs, off, u, ta, tb);
                float v0 = __uint_as_float(u[0].x) + __uint_as_float(u[1].x), v1 = __uint_as_float(u[0].z) + __uint_as_float(u[1].z);
                v0 = red32(v0, v0); v1 = red32(v1, v1);
                const float o0 = dpp<ROR8>(v0), o1 = dpp<ROR8>(v1);
                const float g0r = rbf(v0), g1r = rbf(v1);
                const float th0 = 1.f - 2.f / (1.f + __expf(1.5957691216057308f * (g0r + 0.044715f * g0r * g0r * g0r)));
                const float th1 = 1.f - 2.f / (1.f + __expf(1.5957691216057308f * (g1r + 0.044715f * g1r * g1r * g1r)));
                f32x2 hv; hv[0] = has ? rbf(rbf(0.5f * g0r * (1.f + th0)) * rbf(o0)) : 0.f; hv[1] = has ? rbf(rbf(0.5f * g1r * (1.f + th1)) * rbf(o1)) : 0.f;
                if (kq2 == 0 && mat == 0) *(LAS f32x2*)(hs + bb * 16 + 2 * cp) = hv;
            }
            BAR();
            const f32x2 ha = *(LAS f32x2*)(hs + 2 * w), hb = *(LAS f32x2*)(hs + 16 + 2 * w);
            LAS float* r4b = (LAS float*)(L + L_C + 65536);
            f32x4 oa0, oa1, ob0, ob1;
#pragma unroll
            for (int e = 0; e < 2; ++e) {
                oa0[2 * e] = ha[0] * bflo(d0[e]) + ha[1] * bflo(d1[e]); oa0[2 * e + 1] = ha[0] * bfhi(d0[e]) + ha[1] * bfhi(d1[e]);
                oa1[2 * e] = ha[0] * bflo(d0[2 + e]) + ha[1] * bflo(d1[2 + e]); oa1[2 * e + 1] = ha[0] * bfhi(d0[2 + e]) + ha[1] * bfhi(d1[2 + e]);
                ob0[2 * e] = hb[0] * bflo(d0[e]) + hb[1] * bflo(d1[e]); ob0[2 * e + 1] = hb[0] * bfhi(d0[e]) + hb[1] * bfhi(d1[e]);
                ob1[2 * e] = hb[0] * bflo(d0[2 + e]) + hb[1] * bflo(d1[2 + e]); ob1[2 * e + 1] = hb[0] * bfhi(d0[2 + e]) + hb[1] * bfhi(d1[2 + e]);
            }
            *(LAS f32x4*)(r4b + (w * 2) * 512 + 8 * lane) = oa0; *(LAS f32x4*)(r4b + (w * 2) * 512 + 8 * lane + 4) = oa1;
            *(LAS f32x4*)(r4b + (w * 2 + 1) * 512 + 8 * lane) = ob0; *(LAS f32x4*)(r4b + (w * 2 + 1) * 512 + 8 * lane + 4) = ob1;
            BAR();
            float v0 = 0.f, v1 = 0.f;
#pragma unroll
            for (int ww = 0; ww < 8; ++ww) { const f32x2 r = *(LAS f32x2*)(r4b + ww * 1024 + 2 * t); v0 += r[0]; v1 += r[1]; }
            put_unit(rs, WS_E6 + ((128 + an) * 512 + t) * 16, __float_as_uint(v0), __float_as_uint(v1), ta, tb);
        }
    }
}

extern "C" void kernel_launch(void* const* d_in, const int* in_sizes, int n_in, void* d_out, int out_size, void* d_ws, size_t ws_size, hipStream_t stream) {
    (void)hipFuncSetAttribute((const void*)mla_kernel, hipFuncAttributeMaxDynamicSharedMemorySize, L_TOTAL);
    mla_kernel<<<NWG, NT, L_TOTAL, stream>>>((const bf16_t*)d_in[0], (const bf16_t*)d_in[1], (const bf16_t*)d_in[2], (const bf16_t*)d_in[3],
        (const bf16_t*)d_in[4], (const bf16_t*)d_in[5], (const bf16_t*)d_in[7], (const bf16_t*)d_in[8], (const bf16_t*)d_in[9], (const bf16_t*)d_in[10],
        (bf16_t*)d_out, (unsigned char*)d_ws);
}
```
